```python
import torch

def _softmax(x, axis=-1):
    return torch.softmax(x, dim=axis)
def _gelu(x):
    return torch.nn.functional.gelu(x, approximate="tanh")
def _silu(x):
    return torch.nn.functional.silu(x)
def _sigmoid(x):
    return torch.sigmoid(x)
def _layernorm(x, axis=-1, eps=1e-5):
    m = x.mean(dim=axis, keepdim=True)
    v = x.var(dim=axis, keepdim=True, unbiased=False)
    return (x - m) / torch.sqrt(v + eps)
def _scatter_add(tgt, idx, src, axis=0):
    return tgt.clone().index_add_(axis, idx, src)
def _segment_sum(src, seg, n):
    out = torch.zeros((n,) + src.shape[1:], dtype=src.dtype)
    return out.index_add_(0, seg, src)
def _pad(x, pw):
    flat = tuple(v for pair in reversed(pw) for v in pair)
    return torch.nn.functional.pad(x, flat)

kernel_name = 'parallel_sum__mha_block_cached__glu_gated_glu__decode'

def setup_inputs():

    _w = (lambda a: (lambda t: t.to(torch.bfloat16) if t.is_floating_point() else t)(torch.as_tensor(a)))
    def _A_setup():
        B, Sq, D = 1, 1, 1024
        H, Sk, Dh = 32, 65536, 128
        return {
            "x": _w((lambda *s: torch.randn(*s, dtype=torch.bfloat16) * 0.05)(B, Sq, D)),
            "k_cache": _w((lambda *s: torch.randn(*s, dtype=torch.bfloat16) * 3.2)(B, H, Sk, Dh)),
            "v_cache": _w((lambda *s: torch.randn(*s, dtype=torch.bfloat16) * 1.6)(B, H, Sk, Dh)),
            "wq": _w((lambda *s: torch.randn(*s, dtype=torch.bfloat16) * 3.2)(D, H * Dh) * (D ** -0.5)),
            "wo": _w((lambda *s: torch.randn(*s, dtype=torch.bfloat16) * 1.6)(H * Dh, D) * ((H * Dh) ** -0.5)),
        }
    _A_in = _A_setup()

    def _B_setup():
        B, S, D, F = 1, 1, 1024, 3584
        return {
            "x": _w((lambda *s: torch.randn(*s, dtype=torch.bfloat16) * 0.05)(B, S, D)),
            "w_gate": _w((lambda *s: torch.randn(*s, dtype=torch.bfloat16) * 0.05)(D, F)),
            "w_up": _w((lambda *s: torch.randn(*s, dtype=torch.bfloat16) * 0.05)(D, F)),
            "w_down": _w((lambda *s: torch.randn(*s, dtype=torch.bfloat16) * 0.05)(F, D)),
        }
    _B_in = _B_setup()

    return {
        "A_x": _A_in["x"],
        "A_k_cache": _A_in["k_cache"],
        "A_v_cache": _A_in["v_cache"],
        "A_wq": _A_in["wq"],
        "A_wo": _A_in["wo"],
        "B_x": _B_in["x"],
        "B_w_gate": _B_in["w_gate"],
        "B_w_up": _B_in["w_up"],
        "B_w_down": _B_in["w_down"],
    }

def reference(A_x, A_k_cache, A_v_cache, A_wq, A_wo, B_x, B_w_gate, B_w_up, B_w_down):
    def _A_ref(x, k_cache, v_cache, wq, wo):
        B, Sq, D = x.shape
        H, Dh = 32, 128
        q = (lambda x, *d: x.permute(*d))((x @ wq).reshape(B, Sq, H, Dh), 0, 2, 1, 3)
        scores = torch.einsum("bhqd,bhkd->bhqk", q, k_cache) * (Dh ** -0.5)
        p = _softmax(scores, axis=-1)
        o = torch.einsum("bhqk,bhkd->bhqd", p, v_cache)
        return (lambda x, *d: x.permute(*d))(o, 0, 2, 1, 3).reshape(B, Sq, H * Dh) @ wo

    def _B_ref(x, w_gate, w_up, w_down):
        g = torch.einsum("bsd,df->bsf", x, w_gate)
        u = torch.einsum("bsd,df->bsf", x, w_up)
        h = _sigmoid(g) * u
        return torch.einsum("bsf,fd->bsd", h, w_down)

    x = A_x
    xn = _layernorm(x, axis=-1)
    return x + _A_ref(xn, A_k_cache, A_v_cache, A_wq, A_wo) + _B_ref(xn, B_w_gate, B_w_up, B_w_down)
```

```cpp
#include <hip/hip_runtime.h>
#include <hip/hip_bf16.h>
#include <stdint.h>

#define MIMIC 1
#define PROF 0

typedef unsigned u32x4 __attribute__((ext_vector_type(4)));
typedef unsigned u32x2 __attribute__((ext_vector_type(2)));
typedef float f32x2 __attribute__((ext_vector_type(2)));
typedef float f32x4 __attribute__((ext_vector_type(4)));
typedef __bf16 bf16x2 __attribute__((ext_vector_type(2)));
typedef unsigned long long u64;

constexpr int DM = 1024, NH = 32, SK = 65536, DH = 128, FF = 3584;
constexpr int NS = 8;
constexpr int NW = 8;
constexpr int NT = NW * 64;
constexpr int NWG = NH * NS;
constexpr int FH = FF / NH;
constexpr int TILES = SK / NS / 16 / NW;
constexpr unsigned OOB = 0x80000000u;
constexpr int AUX_NT = 2;
constexpr float LOG2E = 1.4426950408889634f;
constexpr float SCALE = 0.08838834764831845f;
constexpr int SPIN_MAX = 1 << 18;

constexpr size_t WS_EPOCH = 0;
constexpr size_t G_Q = 32;
constexpr size_t G_G = G_Q + (size_t)NWG * 128;
constexpr size_t G_1 = G_G + (size_t)NWG * 256;
constexpr size_t G_2 = G_1 + (size_t)NWG * 144;
constexpr size_t G_END = G_2 + (size_t)NWG * 128;
constexpr size_t WS_PROF = (G_END * 8 + 4095) / 4096 * 4096;

__device__ __forceinline__ float bf_lo(unsigned u) { return __builtin_bit_cast(float, u << 16); }
__device__ __forceinline__ float bf_hi(unsigned u) { return __builtin_bit_cast(float, u & 0xffff0000u); }
__device__ __forceinline__ unsigned cvtpk(float lo, float hi) {
  bf16x2 r = {(__bf16)lo, (__bf16)hi};
  return __builtin_bit_cast(unsigned, r);
}
__device__ __forceinline__ float rbf(float x) { return bf_lo(cvtpk(x, 0.f)); }
__device__ __forceinline__ float mrbf(float x) { return MIMIC ? rbf(x) : x; }
__device__ __forceinline__ float dot2(unsigned a, unsigned b, float c) {
  return __builtin_amdgcn_fdot2_f32_bf16(__builtin_bit_cast(bf16x2, a), __builtin_bit_cast(bf16x2, b), c, false);
}
template <int CTRL> __device__ __forceinline__ float dpp(float x) {
  return __builtin_bit_cast(float, __builtin_amdgcn_mov_dpp(__builtin_bit_cast(int, x), CTRL, 0xf, 0xf, true));
}
constexpr int XOR1 = 0xB1, XOR2 = 0x4E, HMIR = 0x141, ROR8 = 0x128, ROR4 = 0x124, ROR2 = 0x122, ROR1 = 0x121, BCAST = 0x150;
__device__ __forceinline__ float xrow_sum(float x) {
  auto s = __builtin_amdgcn_permlane16_swap(__float_as_uint(x), __float_as_uint(x), false, false);
  x = __uint_as_float(s[0]) + __uint_as_float(s[1]);
  auto t = __builtin_amdgcn_permlane32_swap(__float_as_uint(x), __float_as_uint(x), false, false);
  return __uint_as_float(t[0]) + __uint_as_float(t[1]);
}
__device__ __forceinline__ float xrow_max(float x) {
  auto s = __builtin_amdgcn_permlane16_swap(__float_as_uint(x), __float_as_uint(x), false, false);
  x = fmaxf(__uint_as_float(s[0]), __uint_as_float(s[1]));
  auto t = __builtin_amdgcn_permlane32_swap(__float_as_uint(x), __float_as_uint(x), false, false);
  return fmaxf(__uint_as_float(t[0]), __uint_as_float(t[1]));
}
__device__ __forceinline__ float wave_sum(float x) {
  x += dpp<ROR8>(x); x += dpp<ROR4>(x); x += dpp<ROR2>(x); x += dpp<ROR1>(x);
  return xrow_sum(x);
}
__device__ __forceinline__ void lds_barrier() {
  __builtin_amdgcn_fence(__ATOMIC_RELEASE, "workgroup", "local");
  __builtin_amdgcn_s_barrier();
  __builtin_amdgcn_fence(__ATOMIC_ACQUIRE, "workgroup", "local");
}
__device__ __forceinline__ void g_put(u64* p, float v, unsigned tag) {
  __hip_atomic_store(p, ((u64)tag << 32) | __float_as_uint(v), __ATOMIC_RELAXED, __HIP_MEMORY_SCOPE_AGENT);
}
__device__ __forceinline__ u64 g_get(const u64* p) { return __hip_atomic_load(p, __ATOMIC_RELAXED, __HIP_MEMORY_SCOPE_AGENT); }
__device__ __forceinline__ bool g_ok(u64 g, unsigned tag) { return (unsigned)(g >> 32) == tag; }
__device__ __forceinline__ float g_val(u64 g) { return __uint_as_float((unsigned)g); }
__device__ __forceinline__ __amdgpu_buffer_rsrc_t rsrc(const void* p, unsigned bytes) {
  return __builtin_amdgcn_make_buffer_rsrc((void*)p, 0, (int)bytes, 0x00020000);
}
template <int AUX> __device__ __forceinline__ u32x4 bl(__amdgpu_buffer_rsrc_t r, unsigned voff, unsigned soff) {
  return __builtin_amdgcn_raw_buffer_load_b128(r, voff, soff, AUX);
}
__device__ __forceinline__ void axpy8(f32x2 (&acc)[4], float a, u32x4 w) {
  const f32x2 aa = {a, a};
#pragma unroll
  for (int k = 0; k < 4; ++k) { const f32x2 v = {bf_lo(w[k]), bf_hi(w[k])}; acc[k] = __builtin_elementwise_fma(v, aa, acc[k]); }
}
#define SB() __builtin_amdgcn_sched_barrier(0)
#define TS(i) do {} while (0)

__global__ void __launch_bounds__(NT, 2)
fused_decode_block(const unsigned short* __restrict__ x, const void* __restrict__ kc, const void* __restrict__ vc,
                   const void* __restrict__ wq, const void* __restrict__ wo, const void* __restrict__ wg,
                   const void* __restrict__ wu, const void* __restrict__ wd, unsigned* __restrict__ out, u64* ws) {
  __shared__ __attribute__((aligned(16))) float sA[NW * 256];
  __shared__ __attribute__((aligned(16))) float sB[NW * 128];
  __shared__ __attribute__((aligned(16))) float sH[128];
  __shared__ __attribute__((aligned(16))) float sM[2 * NW];
  const int tid = threadIdx.x, lane = tid & 63, g = lane >> 4, j = lane & 15;
  const int w = __builtin_amdgcn_readfirstlane(tid >> 6);
  const int b = blockIdx.x, s = b & 7, h = b >> 3;
  TS(0);
  const unsigned tag = __hip_atomic_load((unsigned*)ws, __ATOMIC_RELAXED, __HIP_MEMORY_SCOPE_AGENT) + 1u;
  const u32x4 xa = *(const u32x4*)(x + 16 * lane), xb = *(const u32x4*)(x + 16 * lane + 8);
  const int row0 = 128 * s + 16 * w + 4 * g;
  const u32x2 xr = *(const u32x2*)(x + row0);
  const auto rq = rsrc(wq, DM * NH * DH * 2), rg = rsrc(wg, DM * FF * 2), ru = rsrc(wu, DM * FF * 2);
  const auto rd = rsrc(wd, FF * DM * 2), ro = rsrc(wo, NH * DH * DM * 2);
  const size_t kvoff = ((size_t)h * SK + (size_t)s * (SK / NS)) * DH * 2;
  const auto rk = rsrc((const char*)kc + kvoff, SK / NS * DH * 2), rv = rsrc((const char*)vc + kvoff, SK / NS * DH * 2);
  u32x4 Wq[4], Wg[4], Wu[4];
  {
    const unsigned oq = row0 * (NH * DH * 2) + h * 256 + j * 16;
#pragma unroll
    for (int t = 0; t < 4; ++t) Wq[t] = bl<AUX_NT>(rq, oq, t * NH * DH * 2);
    const unsigned og = j < 14 ? row0 * (FF * 2) + h * (FH * 2) + j * 16 : OOB;
#pragma unroll
    for (int t = 0; t < 4; ++t) { Wg[t] = bl<AUX_NT>(rg, og, t * FF * 2); Wu[t] = bl<AUX_NT>(ru, og, t * FF * 2); }
  }
  SB();
  float xn[4];
  {
    float v[16];
#pragma unroll
    for (int k = 0; k < 4; ++k) { v[2 * k] = bf_lo(xa[k]); v[2 * k + 1] = bf_hi(xa[k]); v[8 + 2 * k] = bf_lo(xb[k]); v[9 + 2 * k] = bf_hi(xb[k]); }
    float sum = 0.f;
#pragma unroll
    for (int k = 0; k < 16; ++k) sum += v[k];
    const float mean = wave_sum(sum) * (1.f / DM);
    float sq = 0.f;
#pragma unroll
    for (int k = 0; k < 16; ++k) { const float d = v[k] - mean; sq = fmaf(d, d, sq); }
    const float var = wave_sum(sq) * (1.f / DM);
    const float mb = mrbf(mean), den = mrbf(__builtin_sqrtf(mrbf(mrbf(var) + 1e-5f)));
    const float xv[4] = {bf_lo(xr[0]), bf_hi(xr[0]), bf_lo(xr[1]), bf_hi(xr[1])};
#pragma unroll
    for (int t = 0; t < 4; ++t) xn[t] = mrbf(mrbf(xv[t] - mb) / den);
  }
  TS(1);
  {
    f32x2 a[4] = {};
#pragma unroll
    for (int t = 0; t < 4; ++t) axpy8(a, xn[t], Wq[t]);
#pragma unroll
    for (int k = 0; k < 4; ++k) { a[k].x = xrow_sum(a[k].x); a[k].y = xrow_sum(a[k].y); }
    if (g < 2) *(f32x4*)(sB + w * 128 + 8 * j + 4 * g) = g == 0 ? f32x4{a[0].x, a[0].y, a[1].x, a[1].y} : f32x4{a[2].x, a[2].y, a[3].x, a[3].y};
  }
  lds_barrier();
  TS(2);
  if (tid < 128) {
    float t = 0.f;
#pragma unroll
    for (int k = 0; k < NW; ++k) t += sB[k * 128 + tid];
    g_put(ws + G_Q + (size_t)b * 128 + tid, t, tag);
  }
  SB();
  u32x4 K0[4], V0[4], K1[4], V1[4];
  const unsigned kvo = lane * 16;
#define ISSUE(KK, VV, so) do { _Pragma("unroll") for (int t = 0; t < 4; ++t) KK[t] = bl<AUX_NT>(rk, kvo + t * 1024, (so)); \
                               _Pragma("unroll") for (int t = 0; t < 4; ++t) VV[t] = bl<AUX_NT>(rv, kvo + t * 1024, (so)); SB(); } while (0)
  unsigned soff = w * 4096;
  ISSUE(K0, V0, soff); soff += NW * 4096;
  {
    f32x2 a[4] = {}, c[4] = {};
#pragma unroll
    for (int t = 0; t < 4; ++t) { axpy8(a, xn[t], Wg[t]); axpy8(c, xn[t], Wu[t]); }
#pragma unroll
    for (int k = 0; k < 4; ++k) { a[k].x = xrow_sum(a[k].x); a[k].y = xrow_sum(a[k].y); c[k].x = xrow_sum(c[k].x); c[k].y = xrow_sum(c[k].y); }
    const f32x4 val = g == 0 ? f32x4{a[0].x, a[0].y, a[1].x, a[1].y} : g == 1 ? f32x4{a[2].x, a[2].y, a[3].x, a[3].y}
                    : g == 2 ? f32x4{c[0].x, c[0].y, c[1].x, c[1].y} : f32x4{c[2].x, c[2].y, c[3].x, c[3].y};
    *(f32x4*)(sA + w * 256 + (g >> 1) * 128 + 8 * j + 4 * (g & 1)) = val;
  }
  lds_barrier();
  TS(3);
  if (tid < 256) {
    float t = 0.f;
#pragma unroll
    for (int k = 0; k < NW; ++k) t += sA[k * 256 + tid];
    g_put(ws + G_G + (size_t)b * 256 + tid, t, tag);
  }
  SB();
  u32x4 Wd[4], Wo[4];
  {
    const u64* p = ws + G_Q + ((size_t)h * NS + w) * 128 + lane;
    u64 a0 = g_get(p), a1 = g_get(p + 64);
    SB();
    const int rl = 4 * g;
    const unsigned od = (h * FH + 14 * w + rl) * (DM * 2) + s * 256 + j * 16;
#pragma unroll
    for (int t = 0; t < 4; ++t) Wd[t] = bl<AUX_NT>(rd, rl + t < 14 ? od : OOB, t * DM * 2);
    const unsigned oo = (h * DH + 16 * w + rl) * (DM * 2) + s * 256 + j * 16;
#pragma unroll
    for (int t = 0; t < 4; ++t) Wo[t] = bl<AUX_NT>(ro, oo, t * DM * 2);
    SB();
    ISSUE(K1, V1, soff); soff += NW * 4096;
    for (int it = 0; it < SPIN_MAX; ++it) {
      if (__all(g_ok(a0, tag) && g_ok(a1, tag))) break;
      __builtin_amdgcn_s_sleep(2);
      a0 = g_get(p); a1 = g_get(p + 64);
    }
    sB[w * 128 + lane] = g_val(a0); sB[w * 128 + 64 + lane] = g_val(a1);
  }
  lds_barrier();
  TS(4);
  u32x4 q;
  {
    f32x4 lo = {}, hi = {};
#pragma unroll
    for (int k = 0; k < NS; ++k) { lo += *(const f32x4*)(sB + k * 128 + 8 * j); hi += *(const f32x4*)(sB + k * 128 + 8 * j + 4); }
    q = u32x4{cvtpk(lo[0], lo[1]), cvtpk(lo[2], lo[3]), cvtpk(hi[0], hi[1]), cvtpk(hi[2], hi[3])};
  }
  f32x2 z[4] = {};
  {
    const u64* p = ws + G_G + ((size_t)h * NS + w) * 256 + lane;
    u64 a0, a1, a2, a3;
    for (int it = 0; it < SPIN_MAX; ++it) {
      a0 = g_get(p); a1 = g_get(p + 64); a2 = g_get(p + 128); a3 = g_get(p + 192);
      if (__all(g_ok(a0, tag) && g_ok(a1, tag) && g_ok(a2, tag) && g_ok(a3, tag))) break;
      __builtin_amdgcn_s_sleep(2);
    }
    float* d = sA + w * 256 + lane;
    d[0] = g_val(a0); d[64] = g_val(a1); d[128] = g_val(a2); d[192] = g_val(a3);
    lds_barrier();
    if (tid < 128) {
      float gg = 0.f, uu = 0.f;
#pragma unroll
      for (int k = 0; k < NS; ++k) { gg += sA[k * 256 + tid]; uu += sA[k * 256 + 128 + tid]; }
      gg = mrbf(gg); uu = mrbf(uu);
      const float sg = mrbf(1.f / (1.f + __expf(-gg)));
      sH[tid] = tid < FH ? mrbf(sg * uu) : 0.f;
    }
    lds_barrier();
    const f32x2 h0 = *(const f32x2*)(sH + 14 * w + 4 * g), h1 = *(const f32x2*)(sH + 14 * w + 4 * g + 2);
    axpy8(z, h0.x, Wd[0]); axpy8(z, h0.y, Wd[1]); axpy8(z, h1.x, Wd[2]); axpy8(z, h1.y, Wd[3]);
  }
  TS(5);
  float m = -1e30f, l = 0.f;
  f32x2 acc[4] = {};
  auto tile = [&](const u32x4 (&K)[4], const u32x4 (&V)[4]) {
    float d[4];
#pragma unroll
    for (int t = 0; t < 4; ++t) d[t] = dot2(K[t][3], q[3], dot2(K[t][2], q[2], dot2(K[t][1], q[1], dot2(K[t][0], q[0], 0.f))));
    const float y0 = d[0] + dpp<ROR8>(d[0]), y1 = d[1] + dpp<ROR8>(d[1]), y2 = d[2] + dpp<ROR8>(d[2]), y3 = d[3] + dpp<ROR8>(d[3]);
    const float ya = (j & 8) ? y2 : y0, yb = (j & 8) ? y3 : y1;
    const float za = ya + dpp<HMIR>(ya), zb = yb + dpp<HMIR>(yb);
    float sc = (j & 4) ? zb : za;
    sc += dpp<XOR1>(sc); sc += dpp<XOR2>(sc);
    const float t2 = MIMIC ? rbf(rbf(sc) * SCALE) * LOG2E : sc * (SCALE * LOG2E);
    float pm = fmaxf(t2, dpp<ROR4>(t2)); pm = fmaxf(pm, dpp<ROR8>(pm));
    if (__builtin_amdgcn_ballot_w64(pm > m) != 0) {
      const float mn = fmaxf(m, pm), al = __builtin_amdgcn_exp2f(m - mn);
      const f32x2 aa = {al, al};
      l *= al; m = mn;
#pragma unroll
      for (int k = 0; k < 4; ++k) acc[k] *= aa;
    }
    const float p = __builtin_amdgcn_exp2f(t2 - m);
    l += p;
    axpy8(acc, dpp<BCAST + 0>(p), V[0]); axpy8(acc, dpp<BCAST + 4>(p), V[1]);
    axpy8(acc, dpp<BCAST + 8>(p), V[2]); axpy8(acc, dpp<BCAST + 12>(p), V[3]);
    SB();
  };
  for (int i = 0; i < TILES - 2; i += 2) {
    tile(K0, V0); ISSUE(K0, V0, soff); soff += NW * 4096;
    tile(K1, V1); ISSUE(K1, V1, soff); soff += NW * 4096;
  }
  tile(K0, V0);
  tile(K1, V1);
  TS(6);
  {
    l += dpp<ROR4>(l); l += dpp<ROR8>(l);
    const float mw = xrow_max(m), sc = __builtin_amdgcn_exp2f(m - mw);
    l = xrow_sum(l * sc);
#pragma unroll
    for (int k = 0; k < 4; ++k) { acc[k].x = xrow_sum(acc[k].x * sc); acc[k].y = xrow_sum(acc[k].y * sc); }
    if (g < 2) *(f32x4*)(sB + w * 128 + 8 * j + 4 * g) = g == 0 ? f32x4{acc[0].x, acc[0].y, acc[1].x, acc[1].y} : f32x4{acc[2].x, acc[2].y, acc[3].x, acc[3].y};
    if (lane == 0) { sM[w] = mw; sM[NW + w] = l; }
  }
  lds_barrier();
  if (tid < 192) {
    float M = sM[0];
#pragma unroll
    for (int k = 1; k < NW; ++k) M = fmaxf(M, sM[k]);
    float o = 0.f;
#pragma unroll
    for (int k = 0; k < NW; ++k) o = fmaf(__builtin_amdgcn_exp2f(sM[k] - M), tid < 128 ? sB[k * 128 + tid] : sM[NW + k], o);
    if (tid < 130) g_put(ws + G_1 + (size_t)b * 144 + tid, tid == 128 ? M : o, tag);
  }
  TS(7);
  {
    const u64* p = ws + G_1 + ((size_t)h * NS + w) * 144;
    u64 a0, a1, a2;
    for (int it = 0; it < SPIN_MAX; ++it) {
      a0 = g_get(p + lane); a1 = g_get(p + 64 + lane); a2 = g_get(p + 128 + (lane & 1));
      if (__all(g_ok(a0, tag) && g_ok(a1, tag) && g_ok(a2, tag))) break;
      __builtin_amdgcn_s_sleep(4);
    }
    sA[w * 256 + lane] = g_val(a0); sA[w * 256 + 64 + lane] = g_val(a1);
    if (lane < 2) sA[w * 256 + 128 + lane] = g_val(a2);
  }
  lds_barrier();
  TS(8);
  {
    float M = sA[128];
#pragma unroll
    for (int k = 1; k < NS; ++k) M = fmaxf(M, sA[k * 256 + 128]);
    float L = 0.f; f32x4 o = {};
#pragma unroll
    for (int k = 0; k < NS; ++k) {
      const float wt = __builtin_amdgcn_exp2f(sA[k * 256 + 128] - M);
      L = fmaf(wt, sA[k * 256 + 129], L);
      o += wt * *(const f32x4*)(sA + k * 256 + 16 * w + 4 * g);
    }
    const float inv = 1.f / L;
#pragma unroll
    for (int t = 0; t < 4; ++t) axpy8(z, mrbf(o[t] * inv), Wo[t]);
#pragma unroll
    for (int k = 0; k < 4; ++k) { z[k].x = xrow_sum(z[k].x); z[k].y = xrow_sum(z[k].y); }
    if (g < 2) *(f32x4*)(sB + w * 128 + 8 * j + 4 * g) = g == 0 ? f32x4{z[0].x, z[0].y, z[1].x, z[1].y} : f32x4{z[2].x, z[2].y, z[3].x, z[3].y};
  }
  lds_barrier();
  if (tid < 128) {
    float t = 0.f;
#pragma unroll
    for (int k = 0; k < NW; ++k) t += sB[k * 128 + tid];
    g_put(ws + G_2 + ((size_t)s * NH + h) * 128 + tid, t, tag);
  }
  TS(9);
  if (w == 0) {
    const u64* p = ws + G_2 + ((size_t)s * NH + (lane >> 1)) * 128 + 4 * h + 2 * (lane & 1);
    const unsigned xx = *(const unsigned*)(x + 128 * s + 4 * h + 2 * (lane & 1));
    u64 a0, a1;
    for (int it = 0; it < SPIN_MAX; ++it) {
      a0 = g_get(p); a1 = g_get(p + 1);
      if (__all(g_ok(a0, tag) && g_ok(a1, tag))) break;
      __builtin_amdgcn_s_sleep(2);
    }
    float v0 = g_val(a0), v1 = g_val(a1);
    v0 += dpp<XOR2>(v0); v0 += dpp<ROR4>(v0); v0 += dpp<ROR8>(v0); v0 = xrow_sum(v0);
    v1 += dpp<XOR2>(v1); v1 += dpp<ROR4>(v1); v1 += dpp<ROR8>(v1); v1 = xrow_sum(v1);
    if (lane < 2) out[64 * s + 2 * h + lane] = cvtpk(bf_lo(xx) + v0, bf_hi(xx) + v1);
    if (b == 0 && lane == 0) __hip_atomic_store((unsigned*)ws, tag, __ATOMIC_RELAXED, __HIP_MEMORY_SCOPE_AGENT);
  }
  TS(10);
}

extern "C" void kernel_launch(void* const* d_in, const int* in_sizes, int n_in, void* d_out, int out_size, void* d_ws,
                              size_t ws_size, hipStream_t stream) {
  (void)in_sizes; (void)n_in; (void)out_size; (void)ws_size;
  fused_decode_block<<<NWG, NT, 0, stream>>>((const unsigned short*)d_in[0], d_in[1], d_in[2], d_in[3], d_in[4], d_in[6],
                                              d_in[7], d_in[8], (unsigned*)d_out, (u64*)d_ws);
}
```

```cpp
#include <hip/hip_runtime.h>
#include <hip/hip_bf16.h>
#include <stdint.h>

#define MIMIC 1
#define PROF 0

typedef unsigned u32x4 __attribute__((ext_vector_type(4)));
typedef unsigned u32x2 __attribute__((ext_vector_type(2)));
typedef float f32x2 __attribute__((ext_vector_type(2)));
typedef float f32x4 __attribute__((ext_vector_type(4)));
typedef __bf16 bf16x2 __attribute__((ext_vector_type(2)));
typedef unsigned long long u64;

constexpr int DM = 1024, NH = 32, SK = 65536, DH = 128, FF = 3584;
constexpr int NS = 8;
constexpr int NW = 8;
constexpr int NT = NW * 64;
constexpr int NWG = NH * NS;
constexpr int FH = FF / NH;
constexpr int ROUNDS = SK / NS / 16 / NW;
constexpr unsigned RBYTES = NW * 4096;
constexpr unsigned OOB = 0x80000000u;
constexpr int AUX_NT = 2, AUX_DEV = 16;
constexpr float LOG2E = 1.4426950408889634f;
constexpr float SCALE = 0.08838834764831845f;
constexpr int SPIN_MAX = 1 << 18;

#define DR 5
constexpr size_t G_Q = 32;
constexpr size_t G_G = G_Q + (size_t)NWG * 128;
constexpr size_t G_1 = G_G + (size_t)NWG * 256;
constexpr size_t G_2 = G_1 + (size_t)NWG * 144;
constexpr size_t G_END = G_2 + (size_t)NWG * 128;
constexpr size_t WS_PROF = (G_END * 8 + 4095) / 4096 * 4096;

__device__ __forceinline__ float bf_lo(unsigned u) { return __builtin_bit_cast(float, u << 16); }
__device__ __forceinline__ float bf_hi(unsigned u) { return __builtin_bit_cast(float, u & 0xffff0000u); }
__device__ __forceinline__ unsigned cvtpk(float lo, float hi) {
  bf16x2 r = {(__bf16)lo, (__bf16)hi};
  return __builtin_bit_cast(unsigned, r);
}
__device__ __forceinline__ float rbf(float x) { return bf_lo(cvtpk(x, 0.f)); }
__device__ __forceinline__ float mrbf(float x) { return MIMIC ? rbf(x) : x; }
__device__ __forceinline__ float dot2(unsigned a, unsigned b, float c) {
  return __builtin_amdgcn_fdot2_f32_bf16(__builtin_bit_cast(bf16x2, a), __builtin_bit_cast(bf16x2, b), c, false);
}
template <int CTRL> __device__ __forceinline__ float dpp(float x) {
  return __builtin_bit_cast(float, __builtin_amdgcn_mov_dpp(__builtin_bit_cast(int, x), CTRL, 0xf, 0xf, true));
}
constexpr int XOR1 = 0xB1, XOR2 = 0x4E, HMIR = 0x141, ROR8 = 0x128, ROR4 = 0x124, ROR2 = 0x122, ROR1 = 0x121, BCAST = 0x150;
__device__ __forceinline__ float xrow_sum(float x) {
  auto s = __builtin_amdgcn_permlane16_swap(__float_as_uint(x), __float_as_uint(x), false, false);
  x = __uint_as_float(s[0]) + __uint_as_float(s[1]);
  auto t = __builtin_amdgcn_permlane32_swap(__float_as_uint(x), __float_as_uint(x), false, false);
  return __uint_as_float(t[0]) + __uint_as_float(t[1]);
}
__device__ __forceinline__ float xrow_max(float x) {
  auto s = __builtin_amdgcn_permlane16_swap(__float_as_uint(x), __float_as_uint(x), false, false);
  x = fmaxf(__uint_as_float(s[0]), __uint_as_float(s[1]));
  auto t = __builtin_amdgcn_permlane32_swap(__float_as_uint(x), __float_as_uint(x), false, false);
  return fmaxf(__uint_as_float(t[0]), __uint_as_float(t[1]));
}
__device__ __forceinline__ float wave_sum(float x) {
  x += dpp<ROR8>(x); x += dpp<ROR4>(x); x += dpp<ROR2>(x); x += dpp<ROR1>(x);
  return xrow_sum(x);
}
__device__ __forceinline__ void lds_barrier() {
  __builtin_amdgcn_fence(__ATOMIC_RELEASE, "workgroup", "local");
  __builtin_amdgcn_s_barrier();
  __builtin_amdgcn_fence(__ATOMIC_ACQUIRE, "workgroup", "local");
}
__device__ __forceinline__ void g_put(u64* p, float v, unsigned tag) {
  __hip_atomic_store(p, ((u64)tag << 32) | __float_as_uint(v), __ATOMIC_RELAXED, __HIP_MEMORY_SCOPE_AGENT);
}
__device__ __forceinline__ bool g_ok(u32x4 g, unsigned tag) { return g[1] == tag && g[3] == tag; }
__device__ __forceinline__ __amdgpu_buffer_rsrc_t rsrc(const void* p, unsigned bytes) {
  return __builtin_amdgcn_make_buffer_rsrc((void*)p, 0, (int)bytes, 0x00020000);
}
template <int AUX> __device__ __forceinline__ u32x4 bl(__amdgpu_buffer_rsrc_t r, unsigned voff, unsigned soff) {
  return __builtin_amdgcn_raw_buffer_load_b128(r, voff, soff, AUX);
}
__device__ __forceinline__ void axpy8(f32x2 (&acc)[4], float a, u32x4 w) {
  const f32x2 aa = {a, a};
#pragma unroll
  for (int k = 0; k < 4; ++k) { const f32x2 v = {bf_lo(w[k]), bf_hi(w[k])}; acc[k] = __builtin_elementwise_fma(v, aa, acc[k]); }
}
__device__ __forceinline__ f32x2 xrow_sum8(const f32x2 (&a)[4]) {
  auto h32 = [](float p, float q) {
    auto s = __builtin_amdgcn_permlane32_swap(__float_as_uint(p), __float_as_uint(q), false, false);
    return __uint_as_float(s[0]) + __uint_as_float(s[1]); };
  auto h16 = [](float p, float q) {
    auto s = __builtin_amdgcn_permlane16_swap(__float_as_uint(p), __float_as_uint(q), false, false);
    return __uint_as_float(s[0]) + __uint_as_float(s[1]); };
  return f32x2{h16(h32(a[0].x, a[2].x), h32(a[1].x, a[3].x)), h16(h32(a[0].y, a[2].y), h32(a[1].y, a[3].y))};
}
__device__ __forceinline__ float sum_p(float x) { x += dpp<ROR2>(x); x += dpp<ROR4>(x); return x + dpp<ROR8>(x); }
#define SB() __builtin_amdgcn_sched_barrier(0)
#define VMWAIT(n) do { __builtin_amdgcn_s_waitcnt(0xF70 | (n)); SB(); } while (0)
#define SPIN2(a, oa, c, oc, slp) do { if (!__all(g_ok(a, tag) && g_ok(c, tag))) { _Pragma("unroll 1") for (int it = 0; it < SPIN_MAX; ++it) { \
    asm volatile("" ::: "memory"); a = bl<AUX_DEV>(rw, oa, 0); c = bl<AUX_DEV>(rw, oc, 0); \
    if (__all(g_ok(a, tag) && g_ok(c, tag))) break; __builtin_amdgcn_s_sleep(slp); } } } while (0)
#define SPIN1(a, oa, slp) do { if (!__all(g_ok(a, tag))) { _Pragma("unroll 1") for (int it = 0; it < SPIN_MAX; ++it) { \
    asm volatile("" ::: "memory"); a = bl<AUX_DEV>(rw, oa, 0); \
    if (__all(g_ok(a, tag))) break; __builtin_amdgcn_s_sleep(slp); } } } while (0)
#define TS(i) do {} while (0)

__global__ void __launch_bounds__(NT, 2)
fused_decode_block(const unsigned short* __restrict__ x, const void* __restrict__ kc, const void* __restrict__ vc,
                   const void* __restrict__ wq, const void* __restrict__ wo, const void* __restrict__ wg,
                   const void* __restrict__ wu, const void* __restrict__ wd, unsigned* __restrict__ out, u64* ws) {
  __shared__ __attribute__((aligned(16))) float sA[NW * 256];
  __shared__ __attribute__((aligned(16))) float sB[NW * 128];
  __shared__ __attribute__((aligned(16))) float sM[2 * NW];
  const int tid = threadIdx.x, lane = tid & 63, g = lane >> 4, j = lane & 15;
  const int w = __builtin_amdgcn_readfirstlane(tid >> 6);
  const int b = blockIdx.x, s = b & 7, h = b >> 3;
  TS(0);
  unsigned tag = *(const __attribute__((address_space(4))) unsigned*)(uintptr_t)ws;
  const u32x4 xa = *(const u32x4*)(x + 16 * lane), xb = *(const u32x4*)(x + 16 * lane + 8);
  const int row0 = 128 * s + 16 * w + 4 * g;
  const u32x2 xr = *(const u32x2*)(x + row0);
  const auto rq = rsrc(wq, DM * NH * DH * 2), rg = rsrc(wg, DM * FF * 2), ru = rsrc(wu, DM * FF * 2);
  const auto rw = rsrc(ws, G_END * 8);
  const char* kh = (const char*)kc + (size_t)h * SK * DH * 2;
  const char* vh = (const char*)vc + (size_t)h * SK * DH * 2;
  const auto rk = rsrc(kh, SK * DH * 2), rv = rsrc(vh, SK * DH * 2);
  u32x4 Wq[4];
  {
    const unsigned oq = row0 * (NH * DH * 2) + h * 256 + j * 16;
#pragma unroll
    for (int t = 0; t < 4; ++t) Wq[t] = bl<AUX_NT>(rq, oq, t * NH * DH * 2);
  }
  SB();
  __builtin_amdgcn_s_barrier();
  SB();
  u32x4 K0[4], V0[4], K[4], V[4];
  const unsigned kvo = lane * 16;
#define ISSUE(XX, rr, so) do { _Pragma("unroll") for (int t = 0; t < 4; ++t) XX[t] = bl<AUX_NT>(rr, kvo + t * 1024, (so)); SB(); } while (0)
  unsigned soff = (s * ROUNDS + (s & 1) * DR) * RBYTES + w * 4096;
  int rem = ((s & 1) ? ROUNDS - DR : ROUNDS + DR) - 2;
  ISSUE(K0, rk, soff);
  float xn[4];
  {
    float v[16];
#pragma unroll
    for (int k = 0; k < 4; ++k) { v[2 * k] = bf_lo(xa[k]); v[2 * k + 1] = bf_hi(xa[k]); v[8 + 2 * k] = bf_lo(xb[k]); v[9 + 2 * k] = bf_hi(xb[k]); }
    float sum = 0.f;
#pragma unroll
    for (int k = 0; k < 16; ++k) sum += v[k];
    const float mean = wave_sum(sum) * (1.f / DM);
    float sq = 0.f;
#pragma unroll
    for (int k = 0; k < 16; ++k) { const float d = v[k] - mean; sq = fmaf(d, d, sq); }
    const float var = wave_sum(sq) * (1.f / DM);
    const float mb = mrbf(mean), den = mrbf(__builtin_sqrtf(mrbf(mrbf(var) + 1e-5f)));
    const float xv[4] = {bf_lo(xr[0]), bf_hi(xr[0]), bf_lo(xr[1]), bf_hi(xr[1])};
#pragma unroll
    for (int t = 0; t < 4; ++t) xn[t] = mrbf(mrbf(xv[t] - mb) / den);
  }
  asm volatile("" : "+s"(tag));
  tag += 1u;
  TS(1);
  SB();
  ISSUE(V0, rv, soff); soff += RBYTES;
  {
    f32x2 a[4] = {};
#pragma unroll
    for (int t = 0; t < 4; ++t) axpy8(a, xn[t], Wq[t]);
    *(f32x2*)(sB + w * 128 + 8 * j + 2 * g) = xrow_sum8(a);
  }
  lds_barrier();
  TS(2);
  if (tid < 128) {
    float t = 0.f;
#pragma unroll
    for (int k = 0; k < NW; ++k) t += sB[k * 128 + tid];
    g_put(ws + G_Q + (size_t)b * 128 + tid, t, tag);
  }
  SB();
  u32x4 Wg[4], Wu[4];
  {
    const unsigned og = j < 14 ? row0 * (FF * 2) + h * (FH * 2) + j * 16 : OOB;
#pragma unroll
    for (int t = 0; t < 4; ++t) { Wg[t] = bl<AUX_NT>(rg, og, t * FF * 2); Wu[t] = bl<AUX_NT>(ru, og, t * FF * 2); }
  }
  SB();
  lds_barrier();
  TS(3);
  const unsigned oq2 = (G_Q + (h * NS + w) * 128 + 2 * lane) * 8;
  u32x4 qa = bl<AUX_DEV>(rw, oq2, 0);
  SB();
  ISSUE(K, rk, soff); ISSUE(V, rv, soff);
  {
    f32x2 a[4] = {}, c[4] = {};
#pragma unroll
    for (int t = 0; t < 4; ++t) { axpy8(a, xn[t], Wg[t]); axpy8(c, xn[t], Wu[t]); }
    *(f32x2*)(sA + w * 256 + 8 * j + 2 * g) = xrow_sum8(a);
    *(f32x2*)(sA + w * 256 + 128 + 8 * j + 2 * g) = xrow_sum8(c);
  }
  SB();
  SPIN1(qa, oq2, 2);
  *(f32x2*)(sB + w * 128 + 2 * lane) = f32x2{__uint_as_float(qa[0]), __uint_as_float(qa[2])};
  lds_barrier();
  TS(4);
  u32x4 q;
  {
    f32x4 lo = {}, hi = {};
#pragma unroll
    for (int k = 0; k < NS; ++k) { lo += *(const f32x4*)(sB + k * 128 + 8 * j); hi += *(const f32x4*)(sB + k * 128 + 8 * j + 4); }
    q = u32x4{cvtpk(lo[0], lo[1]), cvtpk(lo[2], lo[3]), cvtpk(hi[0], hi[1]), cvtpk(hi[2], hi[3])};
  }
  if (tid >= 256) {
    const int u = tid - 256, i = u & 15, col = ((u >> 4) & 1) * 128 + (i < 14 ? 14 * (u >> 5) + i : 127);
    float t = 0.f;
#pragma unroll
    for (int k = 0; k < NW; ++k) t += sA[k * 256 + col];
    g_put(ws + G_G + (size_t)b * 256 + u, t, tag);
  }
  TS(5);
  SB();
  float m = -1e30f, l = 0.f;
  f32x2 acc[4] = {};
  auto dots = [&](const u32x4 (&KK)[4], float (&d)[4]) {
#pragma unroll
    for (int t = 0; t < 4; ++t) d[t] = dot2(KK[t][3], q[3], dot2(KK[t][2], q[2], dot2(KK[t][1], q[1], dot2(KK[t][0], q[0], 0.f))));
  };
  auto soft = [&](const float (&d)[4]) {
    const float y0 = d[0] + dpp<ROR8>(d[0]), y1 = d[1] + dpp<ROR8>(d[1]), y2 = d[2] + dpp<ROR8>(d[2]), y3 = d[3] + dpp<ROR8>(d[3]);
    const float ya = (j & 8) ? y2 : y0, yb = (j & 8) ? y3 : y1;
    const float za = ya + dpp<HMIR>(ya), zb = yb + dpp<HMIR>(yb);
    float sc = (j & 4) ? zb : za;
    sc += dpp<XOR1>(sc); sc += dpp<XOR2>(sc);
    const float t2 = MIMIC ? rbf(rbf(sc) * SCALE) * LOG2E : sc * (SCALE * LOG2E);
    float pm = fmaxf(t2, dpp<ROR4>(t2)); pm = fmaxf(pm, dpp<ROR8>(pm));
    if (__builtin_amdgcn_ballot_w64(pm > m) != 0) {
      const float mn = fmaxf(m, pm), al = __builtin_amdgcn_exp2f(m - mn);
      const f32x2 aa = {al, al};
      l *= al; m = mn;
#pragma unroll
      for (int k = 0; k < 4; ++k) acc[k] *= aa;
    }
    const float p = __builtin_amdgcn_exp2f(t2 - m);
    l += p;
    return p;
  };
  auto pv = [&](float p, const u32x4 (&VV)[4]) {
    axpy8(acc, dpp<BCAST + 0>(p), VV[0]); axpy8(acc, dpp<BCAST + 4>(p), VV[1]);
    axpy8(acc, dpp<BCAST + 8>(p), VV[2]); axpy8(acc, dpp<BCAST + 12>(p), VV[3]);
  };
  { float d[4]; dots(K0, d); pv(soft(d), V0); SB(); }
  const unsigned od = (h * FH + 14 * w + 4 * g) * (DM * 2) + s * 256 + j * 16;
  const unsigned oo = (h * DH + 16 * w + 4 * g) * (DM * 2) + s * 256 + j * 16;
#pragma unroll 1
  for (;; --rem) {
    const bool last = rem == 0;
    soff += RBYTES;
    const unsigned sb = last ? 0u : soff, st = last ? DM * 2 : 1024;
    const auto ra = rsrc(last ? (const char*)wd : kh, last ? FF * DM * 2 : SK * DH * 2);
    const auto rb = rsrc(last ? (const char*)wo : vh, last ? NH * DH * DM * 2 : SK * DH * 2);
    const unsigned va = last ? od : kvo, vb = last ? oo : kvo;
    float d[4];
    dots(K, d);
    asm volatile("" : "+v"(d[0]), "+v"(d[1]), "+v"(d[2]), "+v"(d[3]));
    SB();
#pragma unroll
    for (int t = 0; t < 4; ++t) K[t] = bl<AUX_NT>(ra, va, sb + t * st);
    SB();
    pv(soft(d), V);
    SB();
#pragma unroll
    for (int t = 0; t < 4; ++t) V[t] = bl<AUX_NT>(rb, vb, sb + t * st);
    SB();
    if (last) break;
  }
  const u32x4 (&Wd)[4] = K;
  const u32x4 (&Wo)[4] = V;
  TS(6);
  const unsigned ogg = (G_G + (h * NS + (j >> 1)) * 256 + w * 32 + 4 * g + 2 * (j & 1)) * 8;
  u32x4 ga = bl<AUX_DEV>(rw, ogg, 0), ua = bl<AUX_DEV>(rw, ogg + 128, 0);
  SB();
  {
    l += dpp<ROR4>(l); l += dpp<ROR8>(l);
    const float mw = xrow_max(m), sc = __builtin_amdgcn_exp2f(m - mw);
    l = xrow_sum(l * sc);
    const f32x2 ss = {sc, sc};
#pragma unroll
    for (int k = 0; k < 4; ++k) acc[k] *= ss;
    *(f32x2*)(sB + w * 128 + 8 * j + 2 * g) = xrow_sum8(acc);
    if (lane == 0) { sM[w] = mw; sM[NW + w] = l; }
  }
  lds_barrier();
  TS(7);
  if (tid < 192) {
    float M = sM[0];
#pragma unroll
    for (int k = 1; k < NW; ++k) M = fmaxf(M, sM[k]);
    float o = 0.f;
#pragma unroll
    for (int k = 0; k < NW; ++k) o = fmaf(__builtin_amdgcn_exp2f(sM[k] - M), tid < 128 ? sB[k * 128 + tid] : sM[NW + k], o);
    if (tid < 130) g_put(ws + G_1 + (size_t)b * 144 + tid, tid == 128 ? M : o, tag);
  }
  SB();
  const unsigned o1 = (G_1 + (h * NS + (j >> 1)) * 144) * 8, o1a = o1 + (16 * w + 4 * g + 2 * (j & 1)) * 8, o1m = o1 + 128 * 8;
  u32x4 pa = bl<AUX_DEV>(rw, o1a, 0), pm = bl<AUX_DEV>(rw, o1m, 0);
  SB();
  f32x2 z[4] = {};
  auto quad4 = [&](float a0, float a1, float (&r)[4]) {
    const float b0 = dpp<XOR1>(a0), b1 = dpp<XOR1>(a1);
    r[0] = (j & 1) ? b0 : a0; r[1] = (j & 1) ? b1 : a1; r[2] = (j & 1) ? a0 : b0; r[3] = (j & 1) ? a1 : b1;
  };
  {
    SPIN2(ga, ogg, ua, ogg + 128, 2);
    const float g0 = mrbf(sum_p(__uint_as_float(ga[0]))), g1 = mrbf(sum_p(__uint_as_float(ga[2])));
    const float u0 = mrbf(sum_p(__uint_as_float(ua[0]))), u1 = mrbf(sum_p(__uint_as_float(ua[2])));
    float hv[4];
    quad4(mrbf(mrbf(1.f / (1.f + __expf(-g0))) * u0), mrbf(mrbf(1.f / (1.f + __expf(-g1))) * u1), hv);
#pragma unroll
    for (int t = 0; t < 4; ++t) axpy8(z, hv[t], Wd[t]);
    asm volatile("" : "+v"(z[0]), "+v"(z[1]), "+v"(z[2]), "+v"(z[3]));
  }
  SB();
  SPIN2(pa, o1a, pm, o1m, 4);
  TS(8);
  {
    const float mp = __uint_as_float(pm[0]);
    float M = fmaxf(mp, dpp<ROR2>(mp)); M = fmaxf(M, dpp<ROR4>(M)); M = fmaxf(M, dpp<ROR8>(M));
    const float wt = __builtin_amdgcn_exp2f(mp - M);
    const float inv = 1.f / sum_p(wt * __uint_as_float(pm[2]));
    float ov[4];
    quad4(mrbf(sum_p(wt * __uint_as_float(pa[0])) * inv), mrbf(sum_p(wt * __uint_as_float(pa[2])) * inv), ov);
#pragma unroll
    for (int t = 0; t < 4; ++t) axpy8(z, ov[t], Wo[t]);
    *(f32x2*)(sA + w * 128 + 8 * j + 2 * g) = xrow_sum8(z);
  }
  lds_barrier();
  if (tid < 128) {
    float t = 0.f;
#pragma unroll
    for (int k = 0; k < NW; ++k) t += sA[k * 128 + tid];
    g_put(ws + G_2 + ((size_t)s * NH + h) * 128 + tid, t, tag);
  }
  TS(9);
  if (w == 2) {
    const unsigned o2 = (G_2 + (s * NH + (lane >> 1)) * 128 + 4 * h + 2 * (lane & 1)) * 8;
    const unsigned xx = *(const unsigned*)(x + 128 * s + 4 * h + 2 * (lane & 1));
    u32x4 fa = bl<AUX_DEV>(rw, o2, 0);
    SPIN1(fa, o2, 1);
    float v0 = __uint_as_float(fa[0]), v1 = __uint_as_float(fa[2]);
    v0 = xrow_sum(sum_p(v0)); v1 = xrow_sum(sum_p(v1));
    if (lane < 2) out[64 * s + 2 * h + lane] = cvtpk(bf_lo(xx) + v0, bf_hi(xx) + v1);
    if (b == 0 && lane == 0) __hip_atomic_store((unsigned*)ws, tag, __ATOMIC_RELAXED, __HIP_MEMORY_SCOPE_AGENT);
  }
  TS(10);
}

extern "C" void kernel_launch(void* const* d_in, const int* in_sizes, int n_in, void* d_out, int out_size, void* d_ws,
                              size_t ws_size, hipStream_t stream) {
  (void)in_sizes; (void)n_in; (void)out_size; (void)ws_size;
  fused_decode_block<<<NWG, NT, 0, stream>>>((const unsigned short*)d_in[0], d_in[1], d_in[2], d_in[3], d_in[4], d_in[6],
                                              d_in[7], d_in[8], (unsigned*)d_out, (u64*)d_ws);
}
```

```cpp
#include <hip/hip_runtime.h>
#include <hip/hip_bf16.h>
#include <stdint.h>

#define MIMIC 1
#define PROF 0

typedef unsigned u32x4 __attribute__((ext_vector_type(4)));
typedef unsigned u32x2 __attribute__((ext_vector_type(2)));
typedef float f32x2 __attribute__((ext_vector_type(2)));
typedef float f32x4 __attribute__((ext_vector_type(4)));
typedef __bf16 bf16x2 __attribute__((ext_vector_type(2)));
typedef unsigned long long u64;

constexpr int DM = 1024, NH = 32, SK = 65536, DH = 128, FF = 3584;
constexpr int NS = 8;
constexpr int NW = 8;
constexpr int NT = NW * 64;
constexpr int NWG = NH * NS;
constexpr int FH = FF / NH;
constexpr int ROUNDS = SK / NS / 16 / NW;
constexpr unsigned RBYTES = NW * 4096;
constexpr unsigned OOB = 0x80000000u;
constexpr int AUX_NT = 2, AUX_DEV = 16;
constexpr float LOG2E = 1.4426950408889634f;
constexpr float SCALE = 0.08838834764831845f;
constexpr int SPIN_MAX = 1 << 18;

#define DD 13950
#define T1 800
constexpr unsigned SLOT = DD * 256u / 64;
constexpr size_t G_Q = 32;
constexpr size_t G_G = G_Q + (size_t)NWG * 128;
constexpr size_t G_1 = G_G + (size_t)NWG * 256;
constexpr size_t G_2 = G_1 + (size_t)NWG * 144;
constexpr size_t G_END = G_2 + (size_t)NWG * 128;
constexpr size_t WS_PROF = (G_END * 8 + 4095) / 4096 * 4096;

__device__ __forceinline__ float bf_lo(unsigned u) { return __builtin_bit_cast(float, u << 16); }
__device__ __forceinline__ float bf_hi(unsigned u) { return __builtin_bit_cast(float, u & 0xffff0000u); }
__device__ __forceinline__ unsigned cvtpk(float lo, float hi) {
  bf16x2 r = {(__bf16)lo, (__bf16)hi};
  return __builtin_bit_cast(unsigned, r);
}
__device__ __forceinline__ float rbf(float x) { return bf_lo(cvtpk(x, 0.f)); }
__device__ __forceinline__ float mrbf(float x) { return MIMIC ? rbf(x) : x; }
__device__ __forceinline__ float dot2(unsigned a, unsigned b, float c) {
  return __builtin_amdgcn_fdot2_f32_bf16(__builtin_bit_cast(bf16x2, a), __builtin_bit_cast(bf16x2, b), c, false);
}
template <int CTRL> __device__ __forceinline__ float dpp(float x) {
  return __builtin_bit_cast(float, __builtin_amdgcn_mov_dpp(__builtin_bit_cast(int, x), CTRL, 0xf, 0xf, true));
}
constexpr int XOR1 = 0xB1, XOR2 = 0x4E, HMIR = 0x141, ROR8 = 0x128, ROR4 = 0x124, ROR2 = 0x122, ROR1 = 0x121, BCAST = 0x150;
__device__ __forceinline__ float xrow_sum(float x) {
  auto s = __builtin_amdgcn_permlane16_swap(__float_as_uint(x), __float_as_uint(x), false, false);
  x = __uint_as_float(s[0]) + __uint_as_float(s[1]);
  auto t = __builtin_amdgcn_permlane32_swap(__float_as_uint(x), __float_as_uint(x), false, false);
  return __uint_as_float(t[0]) + __uint_as_float(t[1]);
}
__device__ __forceinline__ float xrow_max(float x) {
  auto s = __builtin_amdgcn_permlane16_swap(__float_as_uint(x), __float_as_uint(x), false, false);
  x = fmaxf(__uint_as_float(s[0]), __uint_as_float(s[1]));
  auto t = __builtin_amdgcn_permlane32_swap(__float_as_uint(x), __float_as_uint(x), false, false);
  return fmaxf(__uint_as_float(t[0]), __uint_as_float(t[1]));
}
__device__ __forceinline__ float wave_sum(float x) {
  x += dpp<ROR8>(x); x += dpp<ROR4>(x); x += dpp<ROR2>(x); x += dpp<ROR1>(x);
  return xrow_sum(x);
}
__device__ __forceinline__ void lds_barrier() {
  __builtin_amdgcn_fence(__ATOMIC_RELEASE, "workgroup", "local");
  __builtin_amdgcn_s_barrier();
  __builtin_amdgcn_fence(__ATOMIC_ACQUIRE, "workgroup", "local");
}
__device__ __forceinline__ void g_put(u64* p, float v, unsigned tag) {
  __hip_atomic_store(p, ((u64)tag << 32) | __float_as_uint(v), __ATOMIC_RELAXED, __HIP_MEMORY_SCOPE_AGENT);
}
__device__ __forceinline__ bool g_ok(u32x4 g, unsigned tag) { return g[1] == tag && g[3] == tag; }
__device__ __forceinline__ __amdgpu_buffer_rsrc_t rsrc(const void* p, unsigned bytes) {
  return __builtin_amdgcn_make_buffer_rsrc((void*)p, 0, (int)bytes, 0x00020000);
}
template <int AUX> __device__ __forceinline__ u32x4 bl(__amdgpu_buffer_rsrc_t r, unsigned voff, unsigned soff) {
  return __builtin_amdgcn_raw_buffer_load_b128(r, voff, soff, AUX);
}
__device__ __forceinline__ void axpy8(f32x2 (&acc)[4], float a, u32x4 w) {
  const f32x2 aa = {a, a};
#pragma unroll
  for (int k = 0; k < 4; ++k) { const f32x2 v = {bf_lo(w[k]), bf_hi(w[k])}; acc[k] = __builtin_elementwise_fma(v, aa, acc[k]); }
}
__device__ __forceinline__ f32x2 xrow_sum8(const f32x2 (&a)[4]) {
  auto h32 = [](float p, float q) {
    auto s = __builtin_amdgcn_permlane32_swap(__float_as_uint(p), __float_as_uint(q), false, false);
    return __uint_as_float(s[0]) + __uint_as_float(s[1]); };
  auto h16 = [](float p, float q) {
    auto s = __builtin_amdgcn_permlane16_swap(__float_as_uint(p), __float_as_uint(q), false, false);
    return __uint_as_float(s[0]) + __uint_as_float(s[1]); };
  return f32x2{h16(h32(a[0].x, a[2].x), h32(a[1].x, a[3].x)), h16(h32(a[0].y, a[2].y), h32(a[1].y, a[3].y))};
}
__device__ __forceinline__ float sum_p(float x) { x += dpp<ROR2>(x); x += dpp<ROR4>(x); return x + dpp<ROR8>(x); }
#define SB() __builtin_amdgcn_sched_barrier(0)
#define VMWAIT(n) do { __builtin_amdgcn_s_waitcnt(0xF70 | (n)); SB(); } while (0)
#define SPIN2(a, oa, c, oc, slp) do { if (!__all(g_ok(a, tag) && g_ok(c, tag))) { _Pragma("unroll 1") for (int it = 0; it < SPIN_MAX; ++it) { \
    asm volatile("" ::: "memory"); a = bl<AUX_DEV>(rw, oa, 0); c = bl<AUX_DEV>(rw, oc, 0); \
    if (__all(g_ok(a, tag) && g_ok(c, tag))) break; __builtin_amdgcn_s_sleep(slp); } } } while (0)
#define SPIN1(a, oa, slp) do { if (!__all(g_ok(a, tag))) { _Pragma("unroll 1") for (int it = 0; it < SPIN_MAX; ++it) { \
    asm volatile("" ::: "memory"); a = bl<AUX_DEV>(rw, oa, 0); \
    if (__all(g_ok(a, tag))) break; __builtin_amdgcn_s_sleep(slp); } } } while (0)
#define TS(i) do {} while (0)

__global__ void __launch_bounds__(NT, 2)
fused_decode_block(const unsigned short* __restrict__ x, const void* __restrict__ kc, const void* __restrict__ vc,
                   const void* __restrict__ wq, const void* __restrict__ wo, const void* __restrict__ wg,
                   const void* __restrict__ wu, const void* __restrict__ wd, unsigned* __restrict__ out, u64* ws) {
  __shared__ __attribute__((aligned(16))) float sA[NW * 256];
  __shared__ __attribute__((aligned(16))) float sB[NW * 128];
  __shared__ __attribute__((aligned(16))) float sM[2 * NW];
  __shared__ __attribute__((aligned(16))) float sT[NW * ROUNDS * 16];
  const int tid = threadIdx.x, lane = tid & 63, g = lane >> 4, j = lane & 15;
  const int w = __builtin_amdgcn_readfirstlane(tid >> 6);
  const int b = blockIdx.x, s = b & 7, h = b >> 3;
  TS(0);
  const unsigned ph = SLOT / 256 * (w * 32 + (h & 3) * 8 + s);
  unsigned dl = ((unsigned)__builtin_amdgcn_s_memrealtime() + T1) * 256u + ph;
  unsigned tag = *(const __attribute__((address_space(4))) unsigned*)(uintptr_t)ws;
  const u32x4 xa = *(const u32x4*)(x + 16 * lane), xb = *(const u32x4*)(x + 16 * lane + 8);
  const int row0 = 128 * s + 16 * w + 4 * g;
  const u32x2 xr = *(const u32x2*)(x + row0);
  const auto rq = rsrc(wq, DM * NH * DH * 2), rg = rsrc(wg, DM * FF * 2), ru = rsrc(wu, DM * FF * 2);
  const auto rw = rsrc(ws, G_END * 8);
  const char* kh = (const char*)kc + (size_t)h * SK * DH * 2;
  const char* vh = (const char*)vc + (size_t)h * SK * DH * 2;
  const auto rk = rsrc(kh, SK * DH * 2);
  u32x4 Wq[4];
  {
    const unsigned oq = row0 * (NH * DH * 2) + h * 256 + j * 16;
#pragma unroll
    for (int t = 0; t < 4; ++t) Wq[t] = bl<AUX_NT>(rq, oq, t * NH * DH * 2);
  }
  SB();
  __builtin_amdgcn_s_barrier();
  SB();
  u32x4 K0[4], K1[4], A[4], B[4];
  const unsigned kvo = lane * 16;
#define ISSUE(XX, rr, so) do { _Pragma("unroll") for (int t = 0; t < 4; ++t) XX[t] = bl<AUX_NT>(rr, kvo + t * 1024, (so)); SB(); } while (0)
  const unsigned s0 = s * ROUNDS * RBYTES + w * 4096;
  constexpr unsigned step = RBYTES;
  constexpr int pairs = ROUNDS / 2;
  ISSUE(K0, rk, s0);
  float xn[4];
  {
    float v[16];
#pragma unroll
    for (int k = 0; k < 4; ++k) { v[2 * k] = bf_lo(xa[k]); v[2 * k + 1] = bf_hi(xa[k]); v[8 + 2 * k] = bf_lo(xb[k]); v[9 + 2 * k] = bf_hi(xb[k]); }
    float sum = 0.f;
#pragma unroll
    for (int k = 0; k < 16; ++k) sum += v[k];
    const float mean = wave_sum(sum) * (1.f / DM);
    float sq = 0.f;
#pragma unroll
    for (int k = 0; k < 16; ++k) { const float d = v[k] - mean; sq = fmaf(d, d, sq); }
    const float var = wave_sum(sq) * (1.f / DM);
    const float mb = mrbf(mean), den = mrbf(__builtin_sqrtf(mrbf(mrbf(var) + 1e-5f)));
    const float xv[4] = {bf_lo(xr[0]), bf_hi(xr[0]), bf_lo(xr[1]), bf_hi(xr[1])};
#pragma unroll
    for (int t = 0; t < 4; ++t) xn[t] = mrbf(mrbf(xv[t] - mb) / den);
  }
  asm volatile("" : "+s"(tag));
  tag += 1u;
  TS(1);
  SB();
  ISSUE(K1, rk, s0 + step);
  {
    f32x2 a[4] = {};
#pragma unroll
    for (int t = 0; t < 4; ++t) axpy8(a, xn[t], Wq[t]);
    *(f32x2*)(sB + w * 128 + 8 * j + 2 * g) = xrow_sum8(a);
  }
  lds_barrier();
  TS(2);
  if (tid < 128) {
    float t = 0.f;
#pragma unroll
    for (int k = 0; k < NW; ++k) t += sB[k * 128 + tid];
    g_put(ws + G_Q + (size_t)b * 128 + tid, t, tag);
  }
  SB();
  u32x4 Wg[4], Wu[4];
  {
    const unsigned og = j < 14 ? row0 * (FF * 2) + h * (FH * 2) + j * 16 : OOB;
#pragma unroll
    for (int t = 0; t < 4; ++t) { Wg[t] = bl<AUX_NT>(rg, og, t * FF * 2); Wu[t] = bl<AUX_NT>(ru, og, t * FF * 2); }
  }
  SB();
  lds_barrier();
  TS(3);
  const unsigned oq2 = (G_Q + (h * NS + w) * 128 + 2 * lane) * 8;
  u32x4 qa = bl<AUX_DEV>(rw, oq2, 0);
  SB();
  ISSUE(A, rk, s0 + 2 * step); ISSUE(B, rk, s0 + 3 * step);
  {
    f32x2 a[4] = {}, c[4] = {};
#pragma unroll
    for (int t = 0; t < 4; ++t) { axpy8(a, xn[t], Wg[t]); axpy8(c, xn[t], Wu[t]); }
    *(f32x2*)(sA + w * 256 + 8 * j + 2 * g) = xrow_sum8(a);
    *(f32x2*)(sA + w * 256 + 128 + 8 * j + 2 * g) = xrow_sum8(c);
  }
  SB();
  SPIN1(qa, oq2, 2);
  *(f32x2*)(sB + w * 128 + 2 * lane) = f32x2{__uint_as_float(qa[0]), __uint_as_float(qa[2])};
  lds_barrier();
  TS(4);
  u32x4 q;
  {
    f32x4 lo = {}, hi = {};
#pragma unroll
    for (int k = 0; k < NS; ++k) { lo += *(const f32x4*)(sB + k * 128 + 8 * j); hi += *(const f32x4*)(sB + k * 128 + 8 * j + 4); }
    q = u32x4{cvtpk(lo[0], lo[1]), cvtpk(lo[2], lo[3]), cvtpk(hi[0], hi[1]), cvtpk(hi[2], hi[3])};
  }
  if (tid >= 256) {
    const int u = tid - 256, i = u & 15, col = ((u >> 4) & 1) * 128 + (i < 14 ? 14 * (u >> 5) + i : 127);
    float t = 0.f;
#pragma unroll
    for (int k = 0; k < NW; ++k) t += sA[k * 256 + col];
    g_put(ws + G_G + (size_t)b * 256 + u, t, tag);
  }
  TS(5);
  SB();
  float m = -1e30f;
  float* st = sT + w * (ROUNDS * 16) + 4 * g;
  auto pace = [&]() {
    while ((int)((unsigned)__builtin_amdgcn_s_memrealtime() * 256u - dl) < 0) __builtin_amdgcn_s_sleep(1);
    dl += SLOT;
    SB();
  };
  auto dots = [&](const u32x4 (&KK)[4], float (&d)[4]) {
#pragma unroll
    for (int t = 0; t < 4; ++t) d[t] = dot2(KK[t][3], q[3], dot2(KK[t][2], q[2], dot2(KK[t][1], q[1], dot2(KK[t][0], q[0], 0.f))));
    asm volatile("" : "+v"(d[0]), "+v"(d[1]), "+v"(d[2]), "+v"(d[3]));
    SB();
  };
  auto score = [&](const float (&d)[4], float* p) {
    const float y0 = d[0] + dpp<ROR8>(d[0]), y1 = d[1] + dpp<ROR8>(d[1]), y2 = d[2] + dpp<ROR8>(d[2]), y3 = d[3] + dpp<ROR8>(d[3]);
    const float ya = (j & 8) ? y2 : y0, yb = (j & 8) ? y3 : y1;
    const float za = ya + dpp<HMIR>(ya), zb = yb + dpp<HMIR>(yb);
    float sc = (j & 4) ? zb : za;
    sc += dpp<XOR1>(sc); sc += dpp<XOR2>(sc);
    const float t2 = MIMIC ? rbf(rbf(sc) * SCALE) * LOG2E : sc * (SCALE * LOG2E);
    m = fmaxf(m, t2);
    if ((j & 3) == 0) p[j >> 2] = t2;
    SB();
  };
  { float d[4]; dots(K0, d); score(d, st); dots(K1, d); score(d, st + 16); }
  {
    unsigned so = s0 + 2 * step;
    float* p = st + 32;
#pragma unroll 1
    for (int rem = pairs - 2;; --rem) {
      const bool last = rem == 0;
      so = last ? s0 : so + 2 * step;
      const auto r = rsrc(last ? vh : kh, SK * DH * 2);
      float d[4], e[4];
      dots(A, d); dots(B, e);
      pace();
      ISSUE(A, r, so); ISSUE(B, r, so + step);
      score(d, p); score(e, p + 16);
      p += 32;
      if (last) break;
    }
  }
  m = fmaxf(m, dpp<ROR4>(m)); m = xrow_max(fmaxf(m, dpp<ROR8>(m)));
  __builtin_amdgcn_wave_barrier();
  SB();
  float l = 0.f;
  f32x2 acc[4] = {};
  const unsigned od = (h * FH + 14 * w + 4 * g) * (DM * 2) + s * 256 + j * 16;
  const unsigned oo = (h * DH + 16 * w + 4 * g) * (DM * 2) + s * 256 + j * 16;
  {
    unsigned so = s0;
    auto pv = [&](const float* p, const u32x4 (&VV)[4]) {
      f32x4 e = *(const f32x4*)p;
#pragma unroll
      for (int t = 0; t < 4; ++t) e[t] = __builtin_amdgcn_exp2f(e[t] - m);
      l += (e[0] + e[1]) + (e[2] + e[3]);
      SB();
#pragma unroll
      for (int t = 0; t < 4; ++t) axpy8(acc, e[t], VV[t]);
      SB();
    };
#pragma unroll 1
    for (int rem = pairs - 1;; --rem) {
      const bool last = rem == 0;
      so += 2 * step;
      const unsigned sa = last ? 0u : so, sb = last ? 0u : so + step, sd = last ? DM * 2 : 1024;
      const auto ra = rsrc(last ? (const char*)wd : vh, last ? FF * DM * 2 : SK * DH * 2);
      const auto rb = rsrc(last ? (const char*)wo : vh, last ? NH * DH * DM * 2 : SK * DH * 2);
      const unsigned va = last ? od : kvo, vb = last ? oo : kvo;
      pv(st, A);
      pv(st + 16, B);
      asm volatile("" : "+v"(acc[0]), "+v"(acc[1]), "+v"(acc[2]), "+v"(acc[3]));
      if (last) dl -= ph;
      pace();
#pragma unroll
      for (int t = 0; t < 4; ++t) A[t] = bl<AUX_NT>(ra, va, sa + t * sd);
#pragma unroll
      for (int t = 0; t < 4; ++t) B[t] = bl<AUX_NT>(rb, vb, sb + t * sd);
      SB();
      st += 32;
      if (last) break;
    }
  }
  const u32x4 (&Wd)[4] = A;
  const u32x4 (&Wo)[4] = B;
  TS(6);
  const unsigned ogg = (G_G + (h * NS + (j >> 1)) * 256 + w * 32 + 4 * g + 2 * (j & 1)) * 8;
  u32x4 ga = bl<AUX_DEV>(rw, ogg, 0), ua = bl<AUX_DEV>(rw, ogg + 128, 0);
  SB();
  {
    l = xrow_sum(l);
    *(f32x2*)(sB + w * 128 + 8 * j + 2 * g) = xrow_sum8(acc);
    if (lane == 0) { sM[w] = m; sM[NW + w] = l; }
  }
  lds_barrier();
  TS(7);
  if (tid < 192) {
    float M = sM[0];
#pragma unroll
    for (int k = 1; k < NW; ++k) M = fmaxf(M, sM[k]);
    float o = 0.f;
#pragma unroll
    for (int k = 0; k < NW; ++k) o = fmaf(__builtin_amdgcn_exp2f(sM[k] - M), tid < 128 ? sB[k * 128 + tid] : sM[NW + k], o);
    if (tid < 130) g_put(ws + G_1 + (size_t)b * 144 + tid, tid == 128 ? M : o, tag);
  }
  SB();
  const unsigned o1 = (G_1 + (h * NS + (j >> 1)) * 144) * 8, o1a = o1 + (16 * w + 4 * g + 2 * (j & 1)) * 8, o1m = o1 + 128 * 8;
  u32x4 pa = bl<AUX_DEV>(rw, o1a, 0), pm = bl<AUX_DEV>(rw, o1m, 0);
  SB();
  f32x2 z[4] = {};
  auto quad4 = [&](float a0, float a1, float (&r)[4]) {
    const float b0 = dpp<XOR1>(a0), b1 = dpp<XOR1>(a1);
    r[0] = (j & 1) ? b0 : a0; r[1] = (j & 1) ? b1 : a1; r[2] = (j & 1) ? a0 : b0; r[3] = (j & 1) ? a1 : b1;
  };
  {
    SPIN2(ga, ogg, ua, ogg + 128, 2);
    const float g0 = mrbf(sum_p(__uint_as_float(ga[0]))), g1 = mrbf(sum_p(__uint_as_float(ga[2])));
    const float u0 = mrbf(sum_p(__uint_as_float(ua[0]))), u1 = mrbf(sum_p(__uint_as_float(ua[2])));
    float hv[4];
    quad4(mrbf(mrbf(1.f / (1.f + __expf(-g0))) * u0), mrbf(mrbf(1.f / (1.f + __expf(-g1))) * u1), hv);
#pragma unroll
    for (int t = 0; t < 4; ++t) axpy8(z, hv[t], Wd[t]);
    asm volatile("" : "+v"(z[0]), "+v"(z[1]), "+v"(z[2]), "+v"(z[3]));
  }
  SB();
  SPIN2(pa, o1a, pm, o1m, 4);
  TS(8);
  {
    const float mp = __uint_as_float(pm[0]);
    float M = fmaxf(mp, dpp<ROR2>(mp)); M = fmaxf(M, dpp<ROR4>(M)); M = fmaxf(M, dpp<ROR8>(M));
    const float wt = __builtin_amdgcn_exp2f(mp - M);
    const float inv = 1.f / sum_p(wt * __uint_as_float(pm[2]));
    float ov[4];
    quad4(mrbf(sum_p(wt * __uint_as_float(pa[0])) * inv), mrbf(sum_p(wt * __uint_as_float(pa[2])) * inv), ov);
#pragma unroll
    for (int t = 0; t < 4; ++t) axpy8(z, ov[t], Wo[t]);
    *(f32x2*)(sA + w * 128 + 8 * j + 2 * g) = xrow_sum8(z);
  }
  lds_barrier();
  if (tid < 128) {
    float t = 0.f;
#pragma unroll
    for (int k = 0; k < NW; ++k) t += sA[k * 128 + tid];
    g_put(ws + G_2 + ((size_t)s * NH + h) * 128 + tid, t, tag);
  }
  TS(9);
  if (w == 2) {
    const unsigned o2 = (G_2 + (s * NH + (lane >> 1)) * 128 + 4 * h + 2 * (lane & 1)) * 8;
    const unsigned xx = *(const unsigned*)(x + 128 * s + 4 * h + 2 * (lane & 1));
    u32x4 fa = bl<AUX_DEV>(rw, o2, 0);
    SPIN1(fa, o2, 1);
    float v0 = __uint_as_float(fa[0]), v1 = __uint_as_float(fa[2]);
    v0 = xrow_sum(sum_p(v0)); v1 = xrow_sum(sum_p(v1));
    if (lane < 2) out[64 * s + 2 * h + lane] = cvtpk(bf_lo(xx) + v0, bf_hi(xx) + v1);
    if (b == 0 && lane == 0) __hip_atomic_store((unsigned*)ws, tag, __ATOMIC_RELAXED, __HIP_MEMORY_SCOPE_AGENT);
  }
  TS(10);
}

extern "C" void kernel_launch(void* const* d_in, const int* in_sizes, int n_in, void* d_out, int out_size, void* d_ws,
                              size_t ws_size, hipStream_t stream) {
  (void)in_sizes; (void)n_in; (void)out_size; (void)ws_size;
  fused_decode_block<<<NWG, NT, 0, stream>>>((const unsigned short*)d_in[0], d_in[1], d_in[2], d_in[3], d_in[4], d_in[6],
                                              d_in[7], d_in[8], (unsigned*)d_out, (u64*)d_ws);
}
```

```cpp
#define PROF 1
#define QQ 0
#define QZ 1
#define QMIN 0
#define DD 14000
#include <hip/hip_runtime.h>
#include <hip/hip_bf16.h>
#include <stdint.h>

#define MIMIC 1

typedef unsigned u32x4 __attribute__((ext_vector_type(4)));
typedef unsigned u32x2 __attribute__((ext_vector_type(2)));
typedef float f32x2 __attribute__((ext_vector_type(2)));
typedef float f32x4 __attribute__((ext_vector_type(4)));
typedef __bf16 bf16x2 __attribute__((ext_vector_type(2)));
typedef unsigned long long u64;

constexpr int DM = 1024, NH = 32, SK = 65536, DH = 128, FF = 3584;
constexpr int NS = 8;
constexpr int NW = 8;
constexpr int NT = NW * 64;
constexpr int NWG = NH * NS;
constexpr int FH = FF / NH;
constexpr int ROUNDS = SK / NS / 16 / NW;
constexpr unsigned RBYTES = NW * 4096;
constexpr unsigned OOB = 0x80000000u;
#define AUXK 2
#define AUXV 2
constexpr int AUX_NT = 2, AUX_DEV = 16;
constexpr float LOG2E = 1.4426950408889634f;
constexpr float SCALE = 0.08838834764831845f;
constexpr int SPIN_MAX = 1 << 18;

#define T1 800
constexpr unsigned SLOT = DD * 256u / 64, T1_ = T1;
#define XARGS
#define XVALS
constexpr size_t G_Q = 32;
constexpr size_t G_G = G_Q + (size_t)NWG * 128;
constexpr size_t G_1 = G_G + (size_t)NWG * 256;
constexpr size_t G_2 = G_1 + (size_t)NWG * 144;
constexpr size_t G_END = G_2 + (size_t)NWG * 128;
constexpr size_t WS_PROF = (G_END * 8 + 4095) / 4096 * 4096;

__device__ __forceinline__ float bf_lo(unsigned u) { return __builtin_bit_cast(float, u << 16); }
__device__ __forceinline__ float bf_hi(unsigned u) { return __builtin_bit_cast(float, u & 0xffff0000u); }
__device__ __forceinline__ unsigned cvtpk(float lo, float hi) {
  bf16x2 r = {(__bf16)lo, (__bf16)hi};
  return __builtin_bit_cast(unsigned, r);
}
__device__ __forceinline__ float rbf(float x) { return bf_lo(cvtpk(x, 0.f)); }
__device__ __forceinline__ float mrbf(float x) { return MIMIC ? rbf(x) : x; }
__device__ __forceinline__ float dot2(unsigned a, unsigned b, float c) {
  return __builtin_amdgcn_fdot2_f32_bf16(__builtin_bit_cast(bf16x2, a), __builtin_bit_cast(bf16x2, b), c, false);
}
template <int CTRL> __device__ __forceinline__ float dpp(float x) {
  return __builtin_bit_cast(float, __builtin_amdgcn_mov_dpp(__builtin_bit_cast(int, x), CTRL, 0xf, 0xf, true));
}
constexpr int XOR1 = 0xB1, XOR2 = 0x4E, HMIR = 0x141, ROR8 = 0x128, ROR4 = 0x124, ROR2 = 0x122, ROR1 = 0x121, BCAST = 0x150;
__device__ __forceinline__ float xrow_sum(float x) {
  auto s = __builtin_amdgcn_permlane16_swap(__float_as_uint(x), __float_as_uint(x), false, false);
  x = __uint_as_float(s[0]) + __uint_as_float(s[1]);
  auto t = __builtin_amdgcn_permlane32_swap(__float_as_uint(x), __float_as_uint(x), false, false);
  return __uint_as_float(t[0]) + __uint_as_float(t[1]);
}
__device__ __forceinline__ float xrow_max(float x) {
  auto s = __builtin_amdgcn_permlane16_swap(__float_as_uint(x), __float_as_uint(x), false, false);
  x = fmaxf(__uint_as_float(s[0]), __uint_as_float(s[1]));
  auto t = __builtin_amdgcn_permlane32_swap(__float_as_uint(x), __float_as_uint(x), false, false);
  return fmaxf(__uint_as_float(t[0]), __uint_as_float(t[1]));
}
__device__ __forceinline__ float wave_sum(float x) {
  x += dpp<ROR8>(x); x += dpp<ROR4>(x); x += dpp<ROR2>(x); x += dpp<ROR1>(x);
  return xrow_sum(x);
}
__device__ __forceinline__ void lds_barrier() {
  __builtin_amdgcn_fence(__ATOMIC_RELEASE, "workgroup", "local");
  __builtin_amdgcn_s_barrier();
  __builtin_amdgcn_fence(__ATOMIC_ACQUIRE, "workgroup", "local");
}
__device__ __forceinline__ void g_put(u64* p, float v, unsigned tag) {
  __hip_atomic_store(p, ((u64)tag << 32) | __float_as_uint(v), __ATOMIC_RELAXED, __HIP_MEMORY_SCOPE_AGENT);
}
__device__ __forceinline__ bool g_ok(u32x4 g, unsigned tag) { return g[1] == tag && g[3] == tag; }
__device__ __forceinline__ __amdgpu_buffer_rsrc_t rsrc(const void* p, unsigned bytes) {
  return __builtin_amdgcn_make_buffer_rsrc((void*)p, 0, (int)bytes, 0x00020000);
}
template <int AUX> __device__ __forceinline__ u32x4 bl(__amdgpu_buffer_rsrc_t r, unsigned voff, unsigned soff) {
  return __builtin_amdgcn_raw_buffer_load_b128(r, voff, soff, AUX);
}
__device__ __forceinline__ void axpy8(f32x2 (&acc)[4], float a, u32x4 w) {
  const f32x2 aa = {a, a};
#pragma unroll
  for (int k = 0; k < 4; ++k) { const f32x2 v = {bf_lo(w[k]), bf_hi(w[k])}; acc[k] = __builtin_elementwise_fma(v, aa, acc[k]); }
}
__device__ __forceinline__ f32x2 xrow_sum8(const f32x2 (&a)[4]) {
  auto h32 = [](float p, float q) {
    auto s = __builtin_amdgcn_permlane32_swap(__float_as_uint(p), __float_as_uint(q), false, false);
    return __uint_as_float(s[0]) + __uint_as_float(s[1]); };
  auto h16 = [](float p, float q) {
    auto s = __builtin_amdgcn_permlane16_swap(__float_as_uint(p), __float_as_uint(q), false, false);
    return __uint_as_float(s[0]) + __uint_as_float(s[1]); };
  return f32x2{h16(h32(a[0].x, a[2].x), h32(a[1].x, a[3].x)), h16(h32(a[0].y, a[2].y), h32(a[1].y, a[3].y))};
}
__device__ __forceinline__ float sum_p(float x) { x += dpp<ROR2>(x); x += dpp<ROR4>(x); return x + dpp<ROR8>(x); }
#define SB() __builtin_amdgcn_sched_barrier(0)
#define VMWAIT(n) do { __builtin_amdgcn_s_waitcnt(0xF70 | (n)); SB(); } while (0)
#define SPIN2(a, oa, c, oc, slp) do { if (!__all(g_ok(a, tag) && g_ok(c, tag))) { _Pragma("unroll 1") for (int it = 0; it < SPIN_MAX; ++it) { \
    asm volatile("" ::: "memory"); a = bl<AUX_DEV>(rw, oa, 0); c = bl<AUX_DEV>(rw, oc, 0); \
    if (__all(g_ok(a, tag) && g_ok(c, tag))) break; __builtin_amdgcn_s_sleep(slp); } } } while (0)
#define SPIN1(a, oa, slp) do { if (!__all(g_ok(a, tag))) { _Pragma("unroll 1") for (int it = 0; it < SPIN_MAX; ++it) { \
    asm volatile("" ::: "memory"); a = bl<AUX_DEV>(rw, oa, 0); \
    if (__all(g_ok(a, tag))) break; __builtin_amdgcn_s_sleep(slp); } } } while (0)
#define PROF_N 12
#define TS(i) do { SB(); tsv[i] = (unsigned)__builtin_amdgcn_s_memrealtime(); SB(); } while (0)

__global__ void __launch_bounds__(NT, 2)
fused_decode_block(const unsigned short* __restrict__ x, const void* __restrict__ kc, const void* __restrict__ vc,
                   const void* __restrict__ wq, const void* __restrict__ wo, const void* __restrict__ wg,
                   const void* __restrict__ wu, const void* __restrict__ wd, unsigned* __restrict__ out, u64* ws XARGS) {
  __shared__ __attribute__((aligned(16))) float sA[NW * 256];
  __shared__ __attribute__((aligned(16))) float sB[NW * 128];
  __shared__ __attribute__((aligned(16))) float sM[2 * NW];
  __shared__ __attribute__((aligned(16))) float sT[NW * ROUNDS * 16];
  const int tid = threadIdx.x, lane = tid & 63, g = lane >> 4, j = lane & 15;
  const int w = __builtin_amdgcn_readfirstlane(tid >> 6);
  const int b = blockIdx.x, s = b & 7, h = b >> 3;
  unsigned tsv[PROF_N];
  TS(0);
  const unsigned ph = SLOT / 256 * (w * 32 + (h & 3) * 8 + s);
  unsigned dl = ((unsigned)__builtin_amdgcn_s_memrealtime() + T1_) * 256u + ph;
  unsigned tag = *(const __attribute__((address_space(4))) unsigned*)(uintptr_t)ws;
  const u32x4 xa = *(const u32x4*)(x + 16 * lane), xb = *(const u32x4*)(x + 16 * lane + 8);
  const int row0 = 128 * s + 16 * w + 4 * g;
  const u32x2 xr = *(const u32x2*)(x + row0);
  const auto rq = rsrc(wq, DM * NH * DH * 2), rg = rsrc(wg, DM * FF * 2), ru = rsrc(wu, DM * FF * 2);
  const auto rw = rsrc(ws, G_END * 8);
  const char* kh = (const char*)kc + (size_t)h * SK * DH * 2;
  const char* vh = (const char*)vc + (size_t)h * SK * DH * 2;
  const auto rk = rsrc(kh, SK * DH * 2);
  u32x4 Wq[4];
  {
    const unsigned oq = row0 * (NH * DH * 2) + h * 256 + j * 16;
#pragma unroll
    for (int t = 0; t < 4; ++t) Wq[t] = bl<AUX_NT>(rq, oq, t * NH * DH * 2);
  }
  SB();
  __builtin_amdgcn_s_barrier();
  SB();
  u32x4 K0[4], K1[4], A[4], B[4];
  const unsigned kvo = lane * 16;
#define ISSUE(XX, rr, so) do { _Pragma("unroll") for (int t = 0; t < 4; ++t) XX[t] = bl<AUXK>(rr, kvo + t * 1024, (so)); SB(); } while (0)
  const unsigned s0 = s * ROUNDS * RBYTES + w * 4096;
  constexpr unsigned step = RBYTES;
  constexpr int pairs = ROUNDS / 2;
  ISSUE(K0, rk, s0);
  float xn[4];
  {
    float v[16];
#pragma unroll
    for (int k = 0; k < 4; ++k) { v[2 * k] = bf_lo(xa[k]); v[2 * k + 1] = bf_hi(xa[k]); v[8 + 2 * k] = bf_lo(xb[k]); v[9 + 2 * k] = bf_hi(xb[k]); }
    float sum = 0.f;
#pragma unroll
    for (int k = 0; k < 16; ++k) sum += v[k];
    const float mean = wave_sum(sum) * (1.f / DM);
    float sq = 0.f;
#pragma unroll
    for (int k = 0; k < 16; ++k) { const float d = v[k] - mean; sq = fmaf(d, d, sq); }
    const float var = wave_sum(sq) * (1.f / DM);
    const float mb = mrbf(mean), den = mrbf(__builtin_sqrtf(mrbf(mrbf(var) + 1e-5f)));
    const float xv[4] = {bf_lo(xr[0]), bf_hi(xr[0]), bf_lo(xr[1]), bf_hi(xr[1])};
#pragma unroll
    for (int t = 0; t < 4; ++t) xn[t] = mrbf(mrbf(xv[t] - mb) / den);
  }
  asm volatile("" : "+s"(tag));
  tag += 1u;
  TS(1);
  SB();
  ISSUE(K1, rk, s0 + step);
  {
    f32x2 a[4] = {};
#pragma unroll
    for (int t = 0; t < 4; ++t) axpy8(a, xn[t], Wq[t]);
    *(f32x2*)(sB + w * 128 + 8 * j + 2 * g) = xrow_sum8(a);
  }
  lds_barrier();
  TS(2);
  if (tid < 128) {
    float t = 0.f;
#pragma unroll
    for (int k = 0; k < NW; ++k) t += sB[k * 128 + tid];
    g_put(ws + G_Q + (size_t)b * 128 + tid, t, tag);
  }
  SB();
  u32x4 Wg[4], Wu[4];
  {
    const unsigned og = j < 14 ? row0 * (FF * 2) + h * (FH * 2) + j * 16 : OOB;
#pragma unroll
    for (int t = 0; t < 4; ++t) { Wg[t] = bl<AUX_NT>(rg, og, t * FF * 2); Wu[t] = bl<AUX_NT>(ru, og, t * FF * 2); }
  }
  SB();
  lds_barrier();
  TS(3);
  const unsigned oq2 = (G_Q + (h * NS + w) * 128 + 2 * lane) * 8;
  u32x4 qa = bl<AUX_DEV>(rw, oq2, 0);
  SB();
  ISSUE(A, rk, s0 + 2 * step); ISSUE(B, rk, s0 + 3 * step);
  {
    f32x2 a[4] = {}, c[4] = {};
#pragma unroll
    for (int t = 0; t < 4; ++t) { axpy8(a, xn[t], Wg[t]); axpy8(c, xn[t], Wu[t]); }
    *(f32x2*)(sA + w * 256 + 8 * j + 2 * g) = xrow_sum8(a);
    *(f32x2*)(sA + w * 256 + 128 + 8 * j + 2 * g) = xrow_sum8(c);
  }
  SB();
  SPIN1(qa, oq2, 2);
  *(f32x2*)(sB + w * 128 + 2 * lane) = f32x2{__uint_as_float(qa[0]), __uint_as_float(qa[2])};
  lds_barrier();
  TS(4);
  u32x4 q;
  {
    f32x4 lo = {}, hi = {};
#pragma unroll
    for (int k = 0; k < NS; ++k) { lo += *(const f32x4*)(sB + k * 128 + 8 * j); hi += *(const f32x4*)(sB + k * 128 + 8 * j + 4); }
    q = u32x4{cvtpk(lo[0], lo[1]), cvtpk(lo[2], lo[3]), cvtpk(hi[0], hi[1]), cvtpk(hi[2], hi[3])};
  }
  if (tid >= 256) {
    const int u = tid - 256, i = u & 15, col = ((u >> 4) & 1) * 128 + (i < 14 ? 14 * (u >> 5) + i : 127);
    float t = 0.f;
#pragma unroll
    for (int k = 0; k < NW; ++k) t += sA[k * 256 + col];
    g_put(ws + G_G + (size_t)b * 256 + u, t, tag);
  }
  TS(5);
  SB();
  float m = -1e30f;
  float* st = sT + w * (ROUNDS * 16) + 4 * g;
  auto pace = [&]() {
    while ((int)((unsigned)__builtin_amdgcn_s_memrealtime() * 256u - dl) < 0) __builtin_amdgcn_s_sleep(1);
    dl += SLOT;
    SB();
  };
  auto dots = [&](const u32x4 (&KK)[4], float (&d)[4]) {
#pragma unroll
    for (int t = 0; t < 4; ++t) d[t] = dot2(KK[t][3], q[3], dot2(KK[t][2], q[2], dot2(KK[t][1], q[1], dot2(KK[t][0], q[0], 0.f))));
    asm volatile("" : "+v"(d[0]), "+v"(d[1]), "+v"(d[2]), "+v"(d[3]));
    SB();
  };
  auto score = [&](const float (&d)[4], float* p) {
    const float y0 = d[0] + dpp<ROR8>(d[0]), y1 = d[1] + dpp<ROR8>(d[1]), y2 = d[2] + dpp<ROR8>(d[2]), y3 = d[3] + dpp<ROR8>(d[3]);
    const float ya = (j & 8) ? y2 : y0, yb = (j & 8) ? y3 : y1;
    const float za = ya + dpp<HMIR>(ya), zb = yb + dpp<HMIR>(yb);
    float sc = (j & 4) ? zb : za;
    sc += dpp<XOR1>(sc); sc += dpp<XOR2>(sc);
    const float t2 = MIMIC ? rbf(rbf(sc) * SCALE) * LOG2E : sc * (SCALE * LOG2E);
    m = fmaxf(m, t2);
    if ((j & 3) == 0) p[j >> 2] = t2;
    SB();
  };
  { float d[4]; dots(K0, d); score(d, st); dots(K1, d); score(d, st + 16); }
  {
    unsigned so = s0 + 2 * step;
    float* p = st + 32;
#pragma unroll 1
    for (int rem = pairs - 2;; --rem) {
      const bool last = rem == 0;
      so = last ? s0 : so + 2 * step;
      const auto r = rsrc(last ? vh : kh, SK * DH * 2);
      float d[4], e[4];
      dots(A, d); dots(B, e);
      pace();
      ISSUE(A, r, so); ISSUE(B, r, so + step);
      score(d, p); score(e, p + 16);
      p += 32;
      if (last) break;
    }
  }
  TS(10);
  m = fmaxf(m, dpp<ROR4>(m)); m = xrow_max(fmaxf(m, dpp<ROR8>(m)));
  __builtin_amdgcn_wave_barrier();
  SB();
  float l = 0.f;
  f32x2 acc[4] = {};
  const unsigned od = (h * FH + 14 * w + 4 * g) * (DM * 2) + s * 256 + j * 16;
  const unsigned oo = (h * DH + 16 * w + 4 * g) * (DM * 2) + s * 256 + j * 16;
  {
    unsigned so = s0;
    auto pv = [&](const float* p, const u32x4 (&VV)[4]) {
      f32x4 e = *(const f32x4*)p;
#pragma unroll
      for (int t = 0; t < 4; ++t) e[t] = __builtin_amdgcn_exp2f(e[t] - m);
      l += (e[0] + e[1]) + (e[2] + e[3]);
      SB();
#pragma unroll
      for (int t = 0; t < 4; ++t) axpy8(acc, e[t], VV[t]);
      SB();
    };
#pragma unroll 1
    for (int rem = pairs - 1;; --rem) {
      const bool last = rem == 0;
      so += 2 * step;
      const unsigned sa = last ? 0u : so, sb = last ? 0u : so + step, sd = last ? DM * 2 : 1024;
      const auto ra = rsrc(last ? (const char*)wd : vh, last ? FF * DM * 2 : SK * DH * 2);
      const auto rb = rsrc(last ? (const char*)wo : vh, last ? NH * DH * DM * 2 : SK * DH * 2);
      const unsigned va = last ? od : kvo, vb = last ? oo : kvo;
      pv(st, A);
      pv(st + 16, B);
      asm volatile("" : "+v"(acc[0]), "+v"(acc[1]), "+v"(acc[2]), "+v"(acc[3]));
      if (last) dl -= ph;
      pace();
#pragma unroll
      for (int t = 0; t < 4; ++t) A[t] = bl<AUXV>(ra, va, sa + t * sd);
#pragma unroll
      for (int t = 0; t < 4; ++t) B[t] = bl<AUXV>(rb, vb, sb + t * sd);
      SB();
      st += 32;
      if (last) break;
    }
  }
  const u32x4 (&Wd)[4] = A;
  const u32x4 (&Wo)[4] = B;
  TS(6);
  const unsigned ogg = (G_G + (h * NS + (j >> 1)) * 256 + w * 32 + 4 * g + 2 * (j & 1)) * 8;
  u32x4 ga = bl<AUX_DEV>(rw, ogg, 0), ua = bl<AUX_DEV>(rw, ogg + 128, 0);
  SB();
  {
    l = xrow_sum(l);
    *(f32x2*)(sB + w * 128 + 8 * j + 2 * g) = xrow_sum8(acc);
    if (lane == 0) { sM[w] = m; sM[NW + w] = l; }
  }
  lds_barrier();
  TS(7);
  if (tid < 192) {
    float M = sM[0];
#pragma unroll
    for (int k = 1; k < NW; ++k) M = fmaxf(M, sM[k]);
    float o = 0.f;
#pragma unroll
    for (int k = 0; k < NW; ++k) o = fmaf(__builtin_amdgcn_exp2f(sM[k] - M), tid < 128 ? sB[k * 128 + tid] : sM[NW + k], o);
    if (tid < 130) g_put(ws + G_1 + (size_t)b * 144 + tid, tid == 128 ? M : o, tag);
  }
  SB();
  const unsigned o1 = (G_1 + (h * NS + (j >> 1)) * 144) * 8, o1a = o1 + (16 * w + 4 * g + 2 * (j & 1)) * 8, o1m = o1 + 128 * 8;
  u32x4 pa = bl<AUX_DEV>(rw, o1a, 0), pm = bl<AUX_DEV>(rw, o1m, 0);
  SB();
  f32x2 z[4] = {};
  auto quad4 = [&](float a0, float a1, float (&r)[4]) {
    const float b0 = dpp<XOR1>(a0), b1 = dpp<XOR1>(a1);
    r[0] = (j & 1) ? b0 : a0; r[1] = (j & 1) ? b1 : a1; r[2] = (j & 1) ? a0 : b0; r[3] = (j & 1) ? a1 : b1;
  };
  {
    SPIN2(ga, ogg, ua, ogg + 128, 2);
    const float g0 = mrbf(sum_p(__uint_as_float(ga[0]))), g1 = mrbf(sum_p(__uint_as_float(ga[2])));
    const float u0 = mrbf(sum_p(__uint_as_float(ua[0]))), u1 = mrbf(sum_p(__uint_as_float(ua[2])));
    float hv[4];
    quad4(mrbf(mrbf(1.f / (1.f + __expf(-g0))) * u0), mrbf(mrbf(1.f / (1.f + __expf(-g1))) * u1), hv);
#pragma unroll
    for (int t = 0; t < 4; ++t) axpy8(z, hv[t], Wd[t]);
    asm volatile("" : "+v"(z[0]), "+v"(z[1]), "+v"(z[2]), "+v"(z[3]));
  }
  SB();
  SPIN2(pa, o1a, pm, o1m, 4);
  TS(8);
  {
    const float mp = __uint_as_float(pm[0]);
    float M = fmaxf(mp, dpp<ROR2>(mp)); M = fmaxf(M, dpp<ROR4>(M)); M = fmaxf(M, dpp<ROR8>(M));
    const float wt = __builtin_amdgcn_exp2f(mp - M);
    const float inv = 1.f / sum_p(wt * __uint_as_float(pm[2]));
    float ov[4];
    quad4(mrbf(sum_p(wt * __uint_as_float(pa[0])) * inv), mrbf(sum_p(wt * __uint_as_float(pa[2])) * inv), ov);
#pragma unroll
    for (int t = 0; t < 4; ++t) axpy8(z, ov[t], Wo[t]);
    *(f32x2*)(sA + w * 128 + 8 * j + 2 * g) = xrow_sum8(z);
  }
  lds_barrier();
  if (tid < 128) {
    float t = 0.f;
#pragma unroll
    for (int k = 0; k < NW; ++k) t += sA[k * 128 + tid];
    g_put(ws + G_2 + ((size_t)s * NH + h) * 128 + tid, t, tag);
  }
  TS(9);
  if (w == 2) {
    const unsigned o2 = (G_2 + (s * NH + (lane >> 1)) * 128 + 4 * h + 2 * (lane & 1)) * 8;
    const unsigned xx = *(const unsigned*)(x + 128 * s + 4 * h + 2 * (lane & 1));
    u32x4 fa = bl<AUX_DEV>(rw, o2, 0);
    SPIN1(fa, o2, 1);
    float v0 = __uint_as_float(fa[0]), v1 = __uint_as_float(fa[2]);
    v0 = xrow_sum(sum_p(v0)); v1 = xrow_sum(sum_p(v1));
    if (lane < 2) out[64 * s + 2 * h + lane] = cvtpk(bf_lo(xx) + v0, bf_hi(xx) + v1);
    if (b == 0 && lane == 0) __hip_atomic_store((unsigned*)ws, tag, __ATOMIC_RELAXED, __HIP_MEMORY_SCOPE_AGENT);
  }
  TS(11);
  if (w == 2) {
    if (lane == 0) { g_put(ws + G_END + 2 * b, __uint_as_float(tsv[0]), tag); g_put(ws + G_END + 2 * b + 1, __uint_as_float(tsv[QQ]), tag); }
    if (b == 0) {
      const auto rw = rsrc(ws, (G_END + 512) * 8);
      unsigned mn = 0x7fffffffu, mx = 0; const unsigned base = tsv[0] - 100000u;
      for (int k = 0; k < 4; ++k) { const unsigned o = (G_END + 2 * (k * 64 + lane)) * 8; u32x4 a = bl<AUX_DEV>(rw, o, 0); SPIN1(a, o, 1);
        mn = min(mn, a[0] - base); mx = QMIN ? max(mx, ~(a[2] - base)) : max(mx, a[2] - base); }
      for (int k = 1; k < 64; k *= 2) { mn = min(mn, (unsigned)__shfl_xor((int)mn, k)); mx = max(mx, (unsigned)__shfl_xor((int)mx, k)); }
      if (QMIN) mx = ~mx;
      const unsigned until = base + 20000u + (QZ ? mn : mx);
      while ((int)((unsigned)__builtin_amdgcn_s_memrealtime() - until) < 0) __builtin_amdgcn_s_sleep(1);
    }
  }
}

extern "C" void kernel_launch(void* const* d_in, const int* in_sizes, int n_in, void* d_out, int out_size, void* d_ws,
                              size_t ws_size, hipStream_t stream) {
  (void)in_sizes; (void)n_in; (void)out_size; (void)ws_size;
  fused_decode_block<<<NWG, NT, 0, stream>>>((const unsigned short*)d_in[0], d_in[1], d_in[2], d_in[3], d_in[4], d_in[6],
                                              d_in[7], d_in[8], (unsigned*)d_out, (u64*)d_ws XVALS);
}
```

```cpp
#define PROF 1
#define QQ 0
#define QZ 1
#define QMIN 0
#define DD 14000
#define EX 1
#include <hip/hip_runtime.h>
#include <hip/hip_bf16.h>
#include <stdint.h>

#define MIMIC 1

typedef unsigned u32x4 __attribute__((ext_vector_type(4)));
typedef unsigned u32x2 __attribute__((ext_vector_type(2)));
typedef float f32x2 __attribute__((ext_vector_type(2)));
typedef float f32x4 __attribute__((ext_vector_type(4)));
typedef __bf16 bf16x2 __attribute__((ext_vector_type(2)));
typedef unsigned long long u64;

constexpr int DM = 1024, NH = 32, SK = 65536, DH = 128, FF = 3584;
constexpr int NS = 8;
constexpr int NW = 8;
constexpr int NT = NW * 64;
constexpr int NWG = NH * NS;
constexpr int FH = FF / NH;
constexpr int ROUNDS = SK / NS / 16 / NW;
constexpr unsigned RBYTES = NW * 4096;
constexpr unsigned OOB = 0x80000000u;
#define AUXK 2
#define AUXV 2
constexpr int AUX_NT = 2, AUX_DEV = 16;
constexpr float LOG2E = 1.4426950408889634f;
constexpr float SCALE = 0.08838834764831845f;
constexpr int SPIN_MAX = 1 << 18;

#define T1 800
constexpr unsigned SLOT = DD * 256u / 64, T1_ = T1;
#define XARGS
#define XVALS
constexpr size_t G_Q = 32;
constexpr size_t G_G = G_Q + (size_t)NWG * 128;
constexpr size_t G_1 = G_G + (size_t)NWG * 256;
constexpr size_t G_2 = G_1 + (size_t)NWG * 144;
constexpr size_t G_END = G_2 + (size_t)NWG * 128;
constexpr size_t WS_PROF = (G_END * 8 + 4095) / 4096 * 4096;

__device__ __forceinline__ float bf_lo(unsigned u) { return __builtin_bit_cast(float, u << 16); }
__device__ __forceinline__ float bf_hi(unsigned u) { return __builtin_bit_cast(float, u & 0xffff0000u); }
__device__ __forceinline__ unsigned cvtpk(float lo, float hi) {
  bf16x2 r = {(__bf16)lo, (__bf16)hi};
  return __builtin_bit_cast(unsigned, r);
}
__device__ __forceinline__ float rbf(float x) { return bf_lo(cvtpk(x, 0.f)); }
__device__ __forceinline__ float mrbf(float x) { return MIMIC ? rbf(x) : x; }
__device__ __forceinline__ float dot2(unsigned a, unsigned b, float c) {
  return __builtin_amdgcn_fdot2_f32_bf16(__builtin_bit_cast(bf16x2, a), __builtin_bit_cast(bf16x2, b), c, false);
}
template <int CTRL> __device__ __forceinline__ float dpp(float x) {
  return __builtin_bit_cast(float, __builtin_amdgcn_mov_dpp(__builtin_bit_cast(int, x), CTRL, 0xf, 0xf, true));
}
constexpr int XOR1 = 0xB1, XOR2 = 0x4E, HMIR = 0x141, ROR8 = 0x128, ROR4 = 0x124, ROR2 = 0x122, ROR1 = 0x121, BCAST = 0x150;
__device__ __forceinline__ float xrow_sum(float x) {
  auto s = __builtin_amdgcn_permlane16_swap(__float_as_uint(x), __float_as_uint(x), false, false);
  x = __uint_as_float(s[0]) + __uint_as_float(s[1]);
  auto t = __builtin_amdgcn_permlane32_swap(__float_as_uint(x), __float_as_uint(x), false, false);
  return __uint_as_float(t[0]) + __uint_as_float(t[1]);
}
__device__ __forceinline__ float xrow_max(float x) {
  auto s = __builtin_amdgcn_permlane16_swap(__float_as_uint(x), __float_as_uint(x), false, false);
  x = fmaxf(__uint_as_float(s[0]), __uint_as_float(s[1]));
  auto t = __builtin_amdgcn_permlane32_swap(__float_as_uint(x), __float_as_uint(x), false, false);
  return fmaxf(__uint_as_float(t[0]), __uint_as_float(t[1]));
}
__device__ __forceinline__ float wave_sum(float x) {
  x += dpp<ROR8>(x); x += dpp<ROR4>(x); x += dpp<ROR2>(x); x += dpp<ROR1>(x);
  return xrow_sum(x);
}
__device__ __forceinline__ void lds_barrier() {
  __builtin_amdgcn_fence(__ATOMIC_RELEASE, "workgroup", "local");
  __builtin_amdgcn_s_barrier();
  __builtin_amdgcn_fence(__ATOMIC_ACQUIRE, "workgroup", "local");
}
__device__ __forceinline__ void g_put(u64* p, float v, unsigned tag) {
  __hip_atomic_store(p, ((u64)tag << 32) | __float_as_uint(v), __ATOMIC_RELAXED, __HIP_MEMORY_SCOPE_AGENT);
}
__device__ __forceinline__ bool g_ok(u32x4 g, unsigned tag) { return g[1] == tag && g[3] == tag; }
__device__ __forceinline__ __amdgpu_buffer_rsrc_t rsrc(const void* p, unsigned bytes) {
  return __builtin_amdgcn_make_buffer_rsrc((void*)p, 0, (int)bytes, 0x00020000);
}
template <int AUX> __device__ __forceinline__ u32x4 bl(__amdgpu_buffer_rsrc_t r, unsigned voff, unsigned soff) {
  return __builtin_amdgcn_raw_buffer_load_b128(r, voff, soff, AUX);
}
__device__ __forceinline__ void axpy8(f32x2 (&acc)[4], float a, u32x4 w) {
  const f32x2 aa = {a, a};
#pragma unroll
  for (int k = 0; k < 4; ++k) { const f32x2 v = {bf_lo(w[k]), bf_hi(w[k])}; acc[k] = __builtin_elementwise_fma(v, aa, acc[k]); }
}
__device__ __forceinline__ f32x2 xrow_sum8(const f32x2 (&a)[4]) {
  auto h32 = [](float p, float q) {
    auto s = __builtin_amdgcn_permlane32_swap(__float_as_uint(p), __float_as_uint(q), false, false);
    return __uint_as_float(s[0]) + __uint_as_float(s[1]); };
  auto h16 = [](float p, float q) {
    auto s = __builtin_amdgcn_permlane16_swap(__float_as_uint(p), __float_as_uint(q), false, false);
    return __uint_as_float(s[0]) + __uint_as_float(s[1]); };
  return f32x2{h16(h32(a[0].x, a[2].x), h32(a[1].x, a[3].x)), h16(h32(a[0].y, a[2].y), h32(a[1].y, a[3].y))};
}
__device__ __forceinline__ float sum_p(float x) { x += dpp<ROR2>(x); x += dpp<ROR4>(x); return x + dpp<ROR8>(x); }
#define SB() __builtin_amdgcn_sched_barrier(0)
#define VMWAIT(n) do { __builtin_amdgcn_s_waitcnt(0xF70 | (n)); SB(); } while (0)
#define SPIN2(a, oa, c, oc, slp) do { if (!__all(g_ok(a, tag) && g_ok(c, tag))) { _Pragma("unroll 1") for (int it = 0; it < SPIN_MAX; ++it) { \
    asm volatile("" ::: "memory"); a = bl<AUX_DEV>(rw, oa, 0); c = bl<AUX_DEV>(rw, oc, 0); \
    if (__all(g_ok(a, tag) && g_ok(c, tag))) break; __builtin_amdgcn_s_sleep(slp); } } } while (0)
#define SPIN1(a, oa, slp) do { if (!__all(g_ok(a, tag))) { _Pragma("unroll 1") for (int it = 0; it < SPIN_MAX; ++it) { \
    asm volatile("" ::: "memory"); a = bl<AUX_DEV>(rw, oa, 0); \
    if (__all(g_ok(a, tag))) break; __builtin_amdgcn_s_sleep(slp); } } } while (0)
#define PROF_N 12
#define TS(i) do { SB(); tsv[i] = (unsigned)__builtin_amdgcn_s_memrealtime(); SB(); } while (0)

__global__ void __launch_bounds__(NT, 2)
fused_decode_block(const unsigned short* __restrict__ x, const void* __restrict__ kc, const void* __restrict__ vc,
                   const void* __restrict__ wq, const void* __restrict__ wo, const void* __restrict__ wg,
                   const void* __restrict__ wu, const void* __restrict__ wd, unsigned* __restrict__ out, u64* ws XARGS) {
  __shared__ __attribute__((aligned(16))) float sA[NW * 256];
  __shared__ __attribute__((aligned(16))) float sB[NW * 128];
  __shared__ __attribute__((aligned(16))) float sM[2 * NW];
  __shared__ __attribute__((aligned(16))) float sT[NW * ROUNDS * 16];
  const int tid = threadIdx.x, lane = tid & 63, g = lane >> 4, j = lane & 15;
  const int w = __builtin_amdgcn_readfirstlane(tid >> 6);
  const int b = blockIdx.x, s = b & 7, h = b >> 3;
  unsigned tsv[PROF_N];
  TS(0);
  const unsigned ph = SLOT / 256 * (w * 32 + (h & 3) * 8 + s);
  unsigned dl = ((unsigned)__builtin_amdgcn_s_memrealtime() + T1_) * 256u + ph;
  unsigned tag = *(const __attribute__((address_space(4))) unsigned*)(uintptr_t)ws;
  const u32x4 xa = *(const u32x4*)(x + 16 * lane), xb = *(const u32x4*)(x + 16 * lane + 8);
  const int row0 = 128 * s + 16 * w + 4 * g;
  const u32x2 xr = *(const u32x2*)(x + row0);
  const auto rq = rsrc(wq, DM * NH * DH * 2), rg = rsrc(wg, DM * FF * 2), ru = rsrc(wu, DM * FF * 2);
  const auto rw = rsrc(ws, G_END * 8);
  const char* kh = (const char*)kc + (size_t)h * SK * DH * 2;
  const char* vh = (const char*)vc + (size_t)h * SK * DH * 2;
  const auto rk = rsrc(kh, SK * DH * 2);
  u32x4 Wq[4];
  {
    const unsigned oq = row0 * (NH * DH * 2) + h * 256 + j * 16;
#pragma unroll
    for (int t = 0; t < 4; ++t) Wq[t] = bl<AUX_NT>(rq, oq, t * NH * DH * 2);
  }
  SB();
  __builtin_amdgcn_s_barrier();
  SB();
  u32x4 K0[4], K1[4], A[4], B[4];
  const unsigned kvo = lane * 16;
#define ISSUE(XX, rr, so) do { _Pragma("unroll") for (int t = 0; t < 4; ++t) XX[t] = bl<AUXK>(rr, kvo + t * 1024, (so)); SB(); } while (0)
  const unsigned s0 = s * ROUNDS * RBYTES + w * 4096;
  constexpr unsigned step = RBYTES;
  constexpr int pairs = ROUNDS / 2;
  ISSUE(K0, rk, s0);
  float xn[4];
  {
    float v[16];
#pragma unroll
    for (int k = 0; k < 4; ++k) { v[2 * k] = bf_lo(xa[k]); v[2 * k + 1] = bf_hi(xa[k]); v[8 + 2 * k] = bf_lo(xb[k]); v[9 + 2 * k] = bf_hi(xb[k]); }
    float sum = 0.f;
#pragma unroll
    for (int k = 0; k < 16; ++k) sum += v[k];
    const float mean = wave_sum(sum) * (1.f / DM);
    float sq = 0.f;
#pragma unroll
    for (int k = 0; k < 16; ++k) { const float d = v[k] - mean; sq = fmaf(d, d, sq); }
    const float var = wave_sum(sq) * (1.f / DM);
    const float mb = mrbf(mean), den = mrbf(__builtin_sqrtf(mrbf(mrbf(var) + 1e-5f)));
    const float xv[4] = {bf_lo(xr[0]), bf_hi(xr[0]), bf_lo(xr[1]), bf_hi(xr[1])};
#pragma unroll
    for (int t = 0; t < 4; ++t) xn[t] = mrbf(mrbf(xv[t] - mb) / den);
  }
  asm volatile("" : "+s"(tag));
  tag += 1u;
  TS(1);
  SB();
  ISSUE(K1, rk, s0 + step);
  {
    f32x2 a[4] = {};
#pragma unroll
    for (int t = 0; t < 4; ++t) axpy8(a, xn[t], Wq[t]);
    *(f32x2*)(sB + w * 128 + 8 * j + 2 * g) = xrow_sum8(a);
  }
  lds_barrier();
  TS(2);
  if (tid < 128) {
    float t = 0.f;
#pragma unroll
    for (int k = 0; k < NW; ++k) t += sB[k * 128 + tid];
    g_put(ws + G_Q + (size_t)b * 128 + tid, t, tag);
  }
  SB();
  u32x4 Wg[4], Wu[4];
  {
    const unsigned og = j < 14 ? row0 * (FF * 2) + h * (FH * 2) + j * 16 : OOB;
#pragma unroll
    for (int t = 0; t < 4; ++t) { Wg[t] = bl<AUX_NT>(rg, og, t * FF * 2); Wu[t] = bl<AUX_NT>(ru, og, t * FF * 2); }
  }
  SB();
  lds_barrier();
  TS(3);
  const unsigned oq2 = (G_Q + (h * NS + w) * 128 + 2 * lane) * 8;
  u32x4 qa = bl<AUX_DEV>(rw, oq2, 0);
  SB();
  ISSUE(A, rk, s0 + 2 * step); ISSUE(B, rk, s0 + 3 * step);
  {
    f32x2 a[4] = {}, c[4] = {};
#pragma unroll
    for (int t = 0; t < 4; ++t) { axpy8(a, xn[t], Wg[t]); axpy8(c, xn[t], Wu[t]); }
    *(f32x2*)(sA + w * 256 + 8 * j + 2 * g) = xrow_sum8(a);
    *(f32x2*)(sA + w * 256 + 128 + 8 * j + 2 * g) = xrow_sum8(c);
  }
  SB();
  SPIN1(qa, oq2, 2);
  *(f32x2*)(sB + w * 128 + 2 * lane) = f32x2{__uint_as_float(qa[0]), __uint_as_float(qa[2])};
  lds_barrier();
  TS(4);
  u32x4 q;
  {
    f32x4 lo = {}, hi = {};
#pragma unroll
    for (int k = 0; k < NS; ++k) { lo += *(const f32x4*)(sB + k * 128 + 8 * j); hi += *(const f32x4*)(sB + k * 128 + 8 * j + 4); }
    q = u32x4{cvtpk(lo[0], lo[1]), cvtpk(lo[2], lo[3]), cvtpk(hi[0], hi[1]), cvtpk(hi[2], hi[3])};
  }
  if (tid >= 256) {
    const int u = tid - 256, i = u & 15, col = ((u >> 4) & 1) * 128 + (i < 14 ? 14 * (u >> 5) + i : 127);
    float t = 0.f;
#pragma unroll
    for (int k = 0; k < NW; ++k) t += sA[k * 256 + col];
    g_put(ws + G_G + (size_t)b * 256 + u, t, tag);
  }
  TS(5);
  SB();
  float m = -1e30f;
  float* st = sT + w * (ROUNDS * 16) + 4 * g;
  auto pace = [&]() {
    while ((int)((unsigned)__builtin_amdgcn_s_memrealtime() * 256u - dl) < 0) __builtin_amdgcn_s_sleep(1);
    dl += SLOT;
    SB();
  };
  auto dots = [&](const u32x4 (&KK)[4], float (&d)[4]) {
#pragma unroll
    for (int t = 0; t < 4; ++t) d[t] = dot2(KK[t][3], q[3], dot2(KK[t][2], q[2], dot2(KK[t][1], q[1], dot2(KK[t][0], q[0], 0.f))));
    asm volatile("" : "+v"(d[0]), "+v"(d[1]), "+v"(d[2]), "+v"(d[3]));
    SB();
  };
  auto score = [&](const float (&d)[4], float* p) {
    const float y0 = d[0] + dpp<ROR8>(d[0]), y1 = d[1] + dpp<ROR8>(d[1]), y2 = d[2] + dpp<ROR8>(d[2]), y3 = d[3] + dpp<ROR8>(d[3]);
    const float ya = (j & 8) ? y2 : y0, yb = (j & 8) ? y3 : y1;
    const float za = ya + dpp<HMIR>(ya), zb = yb + dpp<HMIR>(yb);
    float sc = (j & 4) ? zb : za;
    sc += dpp<XOR1>(sc); sc += dpp<XOR2>(sc);
    const float t2 = MIMIC ? rbf(rbf(sc) * SCALE) * LOG2E : sc * (SCALE * LOG2E);
    m = fmaxf(m, t2);
    if ((j & 3) == 0) p[j >> 2] = t2;
    SB();
  };
  { float d[4]; dots(K0, d); score(d, st); dots(K1, d); score(d, st + 16); }
  {
    unsigned so = s0 + 2 * step;
    float* p = st + 32;
#pragma unroll 1
    for (int rem = pairs - 2;; --rem) {
      const bool last = rem == 0;
      so = last ? s0 : so + 2 * step;
      const auto r = rsrc(last ? vh : kh, SK * DH * 2);
      float d[4], e[4];
      dots(A, d); dots(B, e);
      pace();
      ISSUE(A, r, so); ISSUE(B, r, so + step);
      score(d, p); score(e, p + 16);
      p += 32;
      if (last) break;
    }
  }
  TS(10);
  m = fmaxf(m, dpp<ROR4>(m)); m = xrow_max(fmaxf(m, dpp<ROR8>(m)));
  __builtin_amdgcn_wave_barrier();
  SB();
  float l = 0.f;
  f32x2 acc[4] = {};
  const unsigned od = (h * FH + 14 * w + 4 * g) * (DM * 2) + s * 256 + j * 16;
  const unsigned oo = (h * DH + 16 * w + 4 * g) * (DM * 2) + s * 256 + j * 16;
  {
    unsigned so = s0;
    auto pv = [&](const float* p, const u32x4 (&VV)[4]) {
      f32x4 e = *(const f32x4*)p;
#pragma unroll
      for (int t = 0; t < 4; ++t) e[t] = __builtin_amdgcn_exp2f(e[t] - m);
      l += (e[0] + e[1]) + (e[2] + e[3]);
      SB();
#pragma unroll
      for (int t = 0; t < 4; ++t) axpy8(acc, e[t], VV[t]);
      SB();
    };
#pragma unroll 1
    for (int rem = pairs - 1;; --rem) {
      const bool last = rem == 0;
      so += 2 * step;
      const unsigned sa = last ? 0u : so, sb = last ? 0u : so + step, sd = last ? DM * 2 : 1024;
      const auto ra = rsrc(last ? (const char*)wd : vh, last ? FF * DM * 2 : SK * DH * 2);
      const auto rb = rsrc(last ? (const char*)wo : vh, last ? NH * DH * DM * 2 : SK * DH * 2);
      const unsigned va = last ? od : kvo, vb = last ? oo : kvo;
      pv(st, A);
      pv(st + 16, B);
      asm volatile("" : "+v"(acc[0]), "+v"(acc[1]), "+v"(acc[2]), "+v"(acc[3]));
      if (last) dl -= ph;
      pace();
#pragma unroll
      for (int t = 0; t < 4; ++t) A[t] = bl<AUXV>(ra, va, sa + t * sd);
#pragma unroll
      for (int t = 0; t < 4; ++t) B[t] = bl<AUXV>(rb, vb, sb + t * sd);
      SB();
      st += 32;
      if (last) break;
    }
  }
  const u32x4 (&Wd)[4] = A;
  const u32x4 (&Wo)[4] = B;
  TS(6);
  const unsigned ogg = (G_G + (h * NS + (j >> 1)) * 256 + w * 32 + 4 * g + 2 * (j & 1)) * 8;
  u32x4 ga = bl<AUX_DEV>(rw, ogg, 0), ua = bl<AUX_DEV>(rw, ogg + 128, 0);
  SB();
  {
    l = xrow_sum(l);
    *(f32x2*)(sB + w * 128 + 8 * j + 2 * g) = xrow_sum8(acc);
    if (lane == 0) { sM[w] = m; sM[NW + w] = l; }
  }
  lds_barrier();
  TS(7);
  if (tid < 192) {
    float M = sM[0];
#pragma unroll
    for (int k = 1; k < NW; ++k) M = fmaxf(M, sM[k]);
    float o = 0.f;
#pragma unroll
    for (int k = 0; k < NW; ++k) o = fmaf(__builtin_amdgcn_exp2f(sM[k] - M), tid < 128 ? sB[k * 128 + tid] : sM[NW + k], o);
    if (tid < 130) g_put(ws + G_1 + (size_t)b * 144 + tid, tid == 128 ? M : o, tag);
  }
  SB();
  const unsigned o1 = (G_1 + (h * NS + (j >> 1)) * 144) * 8, o1a = o1 + (16 * w + 4 * g + 2 * (j & 1)) * 8, o1m = o1 + 128 * 8;
  u32x4 pa = bl<AUX_DEV>(rw, o1a, 0), pm = bl<AUX_DEV>(rw, o1m, 0);
  SB();
  f32x2 z[4] = {};
  auto quad4 = [&](float a0, float a1, float (&r)[4]) {
    const float b0 = dpp<XOR1>(a0), b1 = dpp<XOR1>(a1);
    r[0] = (j & 1) ? b0 : a0; r[1] = (j & 1) ? b1 : a1; r[2] = (j & 1) ? a0 : b0; r[3] = (j & 1) ? a1 : b1;
  };
  {
    SPIN2(ga, ogg, ua, ogg + 128, 2);
    const float g0 = mrbf(sum_p(__uint_as_float(ga[0]))), g1 = mrbf(sum_p(__uint_as_float(ga[2])));
    const float u0 = mrbf(sum_p(__uint_as_float(ua[0]))), u1 = mrbf(sum_p(__uint_as_float(ua[2])));
    float hv[4];
    quad4(mrbf(mrbf(1.f / (1.f + __expf(-g0))) * u0), mrbf(mrbf(1.f / (1.f + __expf(-g1))) * u1), hv);
#pragma unroll
    for (int t = 0; t < 4; ++t) axpy8(z, hv[t], Wd[t]);
    asm volatile("" : "+v"(z[0]), "+v"(z[1]), "+v"(z[2]), "+v"(z[3]));
  }
  SB();
  SPIN2(pa, o1a, pm, o1m, 4);
  TS(8);
  {
    const float mp = __uint_as_float(pm[0]);
    float M = fmaxf(mp, dpp<ROR2>(mp)); M = fmaxf(M, dpp<ROR4>(M)); M = fmaxf(M, dpp<ROR8>(M));
    const float wt = __builtin_amdgcn_exp2f(mp - M);
    const float inv = 1.f / sum_p(wt * __uint_as_float(pm[2]));
    float ov[4];
    quad4(mrbf(sum_p(wt * __uint_as_float(pa[0])) * inv), mrbf(sum_p(wt * __uint_as_float(pa[2])) * inv), ov);
#pragma unroll
    for (int t = 0; t < 4; ++t) axpy8(z, ov[t], Wo[t]);
    *(f32x2*)(sA + w * 128 + 8 * j + 2 * g) = xrow_sum8(z);
  }
  lds_barrier();
  if (tid < 128) {
    float t = 0.f;
#pragma unroll
    for (int k = 0; k < NW; ++k) t += sA[k * 128 + tid];
    g_put(ws + G_2 + ((size_t)s * NH + h) * 128 + tid, t, tag);
  }
  TS(9);
  if (w == 2) {
    const unsigned o2 = (G_2 + (s * NH + (lane >> 1)) * 128 + 4 * h + 2 * (lane & 1)) * 8;
    const unsigned xx = *(const unsigned*)(x + 128 * s + 4 * h + 2 * (lane & 1));
    u32x4 fa = bl<AUX_DEV>(rw, o2, 0);
    SPIN1(fa, o2, 1);
    float v0 = __uint_as_float(fa[0]), v1 = __uint_as_float(fa[2]);
    v0 = xrow_sum(sum_p(v0)); v1 = xrow_sum(sum_p(v1));
    if (lane < 2) out[64 * s + 2 * h + lane] = cvtpk(bf_lo(xx) + v0, bf_hi(xx) + v1);
    if (b == 0 && lane == 0) __hip_atomic_store((unsigned*)ws, tag, __ATOMIC_RELAXED, __HIP_MEMORY_SCOPE_AGENT);
  }
  TS(11);
  if (w == 2) {
    if (lane == 0) { g_put(ws + G_END + 2 * b, __uint_as_float(tsv[0]), tag); g_put(ws + G_END + 2 * b + 1, __uint_as_float(tsv[QQ]), tag); }
    if (b == 0 && EX != 2 && EX != 4) {
      const auto rw = rsrc(ws, (G_END + 512) * 8);
      unsigned mn = 0x7fffffffu, mx = 0; const unsigned base = tsv[0] - 100000u;
      for (int k = 0; k < 4; ++k) { const unsigned o = (G_END + 2 * (k * 64 + lane)) * 8; u32x4 a = bl<AUX_DEV>(rw, o, 0); SPIN1(a, o, 1);
        mn = min(mn, a[0] - base); mx = QMIN ? max(mx, ~(a[2] - base)) : max(mx, a[2] - base); }
      for (int k = 1; k < 64; k *= 2) { mn = min(mn, (unsigned)__shfl_xor((int)mn, k)); mx = max(mx, (unsigned)__shfl_xor((int)mx, k)); }
      if (QMIN) mx = ~mx;
      const unsigned until = EX == 3 ? *(volatile unsigned*)(ws + G_END + 600) + 20000u : base + 20000u + (QZ ? mn : mx);
      while ((int)((unsigned)__builtin_amdgcn_s_memrealtime() - until) < 0) __builtin_amdgcn_s_sleep(1);
    }
  }
}

__global__ void pre_k(u64* ws) { if (threadIdx.x == 0) *(volatile unsigned*)(ws + G_END + 600) = (unsigned)__builtin_amdgcn_s_memrealtime(); }
__global__ void post_k(u64* ws) {
  const int lane = threadIdx.x;
  unsigned base = (unsigned)ws[G_END] - 100000u, mn = 0x7fffffffu;
  for (int k = 0; k < 4; ++k) mn = min(mn, (unsigned)ws[G_END + 2 * (k * 64 + lane)] - base);
  for (int k = 1; k < 64; k *= 2) mn = min(mn, (unsigned)__shfl_xor((int)mn, k));
  const unsigned until = EX == 4 ? *(volatile unsigned*)(ws + G_END + 600) + 20000u : base + 20000u + mn;
  while ((int)((unsigned)__builtin_amdgcn_s_memrealtime() - until) < 0) __builtin_amdgcn_s_sleep(1);
}
extern "C" void kernel_launch(void* const* d_in, const int* in_sizes, int n_in, void* d_out, int out_size, void* d_ws,
                              size_t ws_size, hipStream_t stream) {
  (void)in_sizes; (void)n_in; (void)out_size; (void)ws_size;
  if (EX == 3 || EX == 4) pre_k<<<1, 64, 0, stream>>>((u64*)d_ws);
  fused_decode_block<<<NWG, NT, 0, stream>>>((const unsigned short*)d_in[0], d_in[1], d_in[2], d_in[3], d_in[4], d_in[6],
                                              d_in[7], d_in[8], (unsigned*)d_out, (u64*)d_ws XVALS);
  if (EX == 2 || EX == 4) post_k<<<1, 64, 0, stream>>>((u64*)d_ws);
}
```

```cpp
#include <hip/hip_runtime.h>
#include <hip/hip_bf16.h>
#include <stdint.h>

#define MIMIC 1
#define PROF 0

typedef unsigned u32x4 __attribute__((ext_vector_type(4)));
typedef unsigned u32x2 __attribute__((ext_vector_type(2)));
typedef float f32x2 __attribute__((ext_vector_type(2)));
typedef float f32x4 __attribute__((ext_vector_type(4)));
typedef __bf16 bf16x2 __attribute__((ext_vector_type(2)));
typedef unsigned long long u64;

constexpr int DM = 1024, NH = 32, SK = 65536, DH = 128, FF = 3584;
constexpr int NS = 8;
constexpr int NW = 8;
constexpr int NT = NW * 64;
constexpr int NWG = NH * NS;
constexpr int FH = FF / NH;
constexpr int ROUNDS = SK / NS / 16 / NW;
constexpr unsigned RBYTES = NW * 4096;
constexpr unsigned OOB = 0x80000000u;
#define AUXK 2
#define AUXV 2
#define AUXP 16
constexpr int AUX_NT = 2, AUX_DEV = 16;
constexpr float LOG2E = 1.4426950408889634f;
constexpr float SCALE = 0.08838834764831845f;
constexpr int SPIN_MAX = 1 << 18;

#define DD 13800
#define TG 150
#define TGW 200
#define TK 620
#define TW0 370
#define TW1 200
#define WSOFF 0
#define TD 80
#define WS 4
#define WE 3
#define TX 0
#define TP1 116
#define TP2 214
#define RM 1
#define TA 12
#define TH 0
#define SG 0
#ifndef CU_
#define CU_ 0
#endif
#define HS 0
#define TY1 0
#define TY2 0
#define PF0 1
#define PF1 0
#define PF2 0
#define WARM 0
#define ORDER 0
#define HP 1
#define GE 30
constexpr unsigned SLOT = DD * 256u / 64, TK_ = TK, TG_ = TG, TW0_ = TW0, TW1_ = TW1, TGW_ = TGW, TD_ = TD, P6 = WE, P7 = TX, P8 = TP1, P9 = TP2, P10 = RM, P11 = TA, P12 = TH, P13 = SG, P14 = CU_, P15 = HS;
#define XARGS
#define XVALS
#define PFA PF0
#define PFB PF1
#define PFC PF2
#define PY1 TY1
#define PY2 TY2
constexpr size_t G_Q = 32;
constexpr size_t G_H = G_Q + NH * 4 * DH;
constexpr size_t G_T = G_H + 4 * FF;
constexpr size_t G_M = G_T + NWG * 2;
constexpr size_t G_1 = G_M + NWG * 2;
constexpr size_t G_2 = G_1 + (size_t)NWG * 128;
constexpr size_t G_END = G_2 + (size_t)NWG * 128;
constexpr size_t WS_PROF = (G_END * 8 + 4095) / 4096 * 4096;

__device__ __forceinline__ float bf_lo(unsigned u) { return __builtin_bit_cast(float, u << 16); }
__device__ __forceinline__ float bf_hi(unsigned u) { return __builtin_bit_cast(float, u & 0xffff0000u); }
__device__ __forceinline__ unsigned cvtpk(float lo, float hi) {
  bf16x2 r = {(__bf16)lo, (__bf16)hi};
  return __builtin_bit_cast(unsigned, r);
}
__device__ __forceinline__ float rbf(float x) { return bf_lo(cvtpk(x, 0.f)); }
__device__ __forceinline__ float mrbf(float x) { return MIMIC ? rbf(x) : x; }
__device__ __forceinline__ float dot2(unsigned a, unsigned b, float c) {
  return __builtin_amdgcn_fdot2_f32_bf16(__builtin_bit_cast(bf16x2, a), __builtin_bit_cast(bf16x2, b), c, false);
}
template <int CTRL> __device__ __forceinline__ float dpp(float x) {
  return __builtin_bit_cast(float, __builtin_amdgcn_mov_dpp(__builtin_bit_cast(int, x), CTRL, 0xf, 0xf, true));
}
constexpr int XOR1 = 0xB1, XOR2 = 0x4E, HMIR = 0x141, ROR8 = 0x128, ROR4 = 0x124, ROR2 = 0x122, ROR1 = 0x121, BCAST = 0x150;
__device__ __forceinline__ float xrow_sum(float x) {
  auto s = __builtin_amdgcn_permlane16_swap(__float_as_uint(x), __float_as_uint(x), false, false);
  x = __uint_as_float(s[0]) + __uint_as_float(s[1]);
  auto t = __builtin_amdgcn_permlane32_swap(__float_as_uint(x), __float_as_uint(x), false, false);
  return __uint_as_float(t[0]) + __uint_as_float(t[1]);
}
__device__ __forceinline__ float xrow_max(float x) {
  auto s = __builtin_amdgcn_permlane16_swap(__float_as_uint(x), __float_as_uint(x), false, false);
  x = fmaxf(__uint_as_float(s[0]), __uint_as_float(s[1]));
  auto t = __builtin_amdgcn_permlane32_swap(__float_as_uint(x), __float_as_uint(x), false, false);
  return fmaxf(__uint_as_float(t[0]), __uint_as_float(t[1]));
}
__device__ __forceinline__ float wave_sum(float x) {
  x += dpp<ROR8>(x); x += dpp<ROR4>(x); x += dpp<ROR2>(x); x += dpp<ROR1>(x);
  return xrow_sum(x);
}
__device__ __forceinline__ void lds_barrier() {
  __builtin_amdgcn_fence(__ATOMIC_RELEASE, "workgroup", "local");
  __builtin_amdgcn_s_barrier();
  __builtin_amdgcn_fence(__ATOMIC_ACQUIRE, "workgroup", "local");
}
__device__ __forceinline__ void g_put(u64* p, float v, unsigned tag) {
  __hip_atomic_store(p, ((u64)tag << 32) | __float_as_uint(v), __ATOMIC_RELAXED, __HIP_MEMORY_SCOPE_AGENT);
}
__device__ __forceinline__ void g_put2(__amdgpu_buffer_rsrc_t r, unsigned granule, unsigned a, unsigned b, unsigned tag) {
  __builtin_amdgcn_raw_buffer_store_b128(u32x4{a, tag, b, tag}, r, granule * 8, 0, AUX_DEV);
}
__device__ __forceinline__ void g_put2(__amdgpu_buffer_rsrc_t r, unsigned granule, f32x2 v, unsigned tag) {
  g_put2(r, granule, __float_as_uint(v.x), __float_as_uint(v.y), tag);
}
__device__ __forceinline__ bool g_ok(u32x4 g, unsigned tag) { return g[1] == tag && g[3] == tag; }
__device__ __forceinline__ __amdgpu_buffer_rsrc_t rsrc(const void* p, unsigned bytes) {
  return __builtin_amdgcn_make_buffer_rsrc((void*)p, 0, (int)bytes, 0x00020000);
}
template <int AUX> __device__ __forceinline__ u32x4 bl(__amdgpu_buffer_rsrc_t r, unsigned voff, unsigned soff) {
  return __builtin_amdgcn_raw_buffer_load_b128(r, voff, soff, AUX);
}
__device__ __forceinline__ void axpy8(f32x2 (&acc)[4], float a, u32x4 w) {
  const f32x2 aa = {a, a};
#pragma unroll
  for (int k = 0; k < 4; ++k) { const f32x2 v = {bf_lo(w[k]), bf_hi(w[k])}; acc[k] = __builtin_elementwise_fma(v, aa, acc[k]); }
}
__device__ __forceinline__ f32x2 xrow_sum8(const f32x2 (&a)[4]) {
  auto h32 = [](float p, float q) {
    auto s = __builtin_amdgcn_permlane32_swap(__float_as_uint(p), __float_as_uint(q), false, false);
    return __uint_as_float(s[0]) + __uint_as_float(s[1]); };
  auto h16 = [](float p, float q) {
    auto s = __builtin_amdgcn_permlane16_swap(__float_as_uint(p), __float_as_uint(q), false, false);
    return __uint_as_float(s[0]) + __uint_as_float(s[1]); };
  return f32x2{h16(h32(a[0].x, a[2].x), h32(a[1].x, a[3].x)), h16(h32(a[0].y, a[2].y), h32(a[1].y, a[3].y))};
}
__device__ __forceinline__ float sum_p(float x) { x += dpp<ROR2>(x); x += dpp<ROR4>(x); return x + dpp<ROR8>(x); }
__device__ __forceinline__ f32x2 merge8(f32x2 v) {
  float M = fmaxf(v.x, dpp<XOR1>(v.x)); M = fmaxf(M, dpp<XOR2>(M)); M = fmaxf(M, dpp<HMIR>(M));
  float L = v.y * __builtin_amdgcn_exp2f(v.x - M);
  L += dpp<XOR1>(L); L += dpp<XOR2>(L); L += dpp<HMIR>(L);
  return f32x2{M, L};
}
#define SB() __builtin_amdgcn_sched_barrier(0)
#define VMWAIT(n) do { __builtin_amdgcn_s_waitcnt(0xF70 | (n)); SB(); } while (0)
#define SPIN2(a, oa, c, oc, slp) do { if (!__all(g_ok(a, tag) && g_ok(c, tag))) { _Pragma("unroll 1") for (int it = 0; it < SPIN_MAX; ++it) { \
    asm volatile("" ::: "memory"); a = bl<AUX_DEV>(rw, oa, 0); c = bl<AUX_DEV>(rw, oc, 0); \
    if (__all(g_ok(a, tag) && g_ok(c, tag))) break; __builtin_amdgcn_s_sleep(slp); } } } while (0)
#define SPIN1(a, oa, slp) do { if (!__all(g_ok(a, tag))) { _Pragma("unroll 1") for (int it = 0; it < SPIN_MAX; ++it) { \
    asm volatile("" ::: "memory"); a = bl<AUXP>(rw, oa, 0); \
    if (__all(g_ok(a, tag))) break; __builtin_amdgcn_s_sleep(slp); } } } while (0)
#define TS(i) do {} while (0)

__global__ void __launch_bounds__(NT, 2)
fused_decode_block(const unsigned short* __restrict__ x, const void* __restrict__ kc, const void* __restrict__ vc,
                   const void* __restrict__ wq, const void* __restrict__ wo, const void* __restrict__ wg,
                   const void* __restrict__ wu, const void* __restrict__ wd, unsigned* __restrict__ out, u64* ws XARGS) {
  __shared__ __attribute__((aligned(16))) float sA[NW * 128];
  __shared__ __attribute__((aligned(16))) float sB[NW * 128];
  __shared__ int sCnt, sCn2, sCn3, sQf;
  __shared__ __attribute__((aligned(16))) unsigned sQ[68];
  __shared__ __attribute__((aligned(16))) float sX[DM];
  __shared__ __attribute__((aligned(16))) float sM[2 * NW];
  __shared__ __attribute__((aligned(16))) float sT[NW * ROUNDS * 16];
  const int tid = threadIdx.x, lane = tid & 63, g = lane >> 4, j = lane & 15;
  const int w = __builtin_amdgcn_readfirstlane(tid >> 6);
  const int b = blockIdx.x, s = b & 7, h = b >> 3;
  const int hc = P10 ? 4 * s + (h & 3) : h, sc = P10 ? h >> 2 : s;
  TS(0);
  const unsigned t0 = (unsigned)__builtin_amdgcn_s_memrealtime() * 256u;
  const unsigned idx = w * 32 + (h & 3) * 8 + s;
  auto until = [](unsigned t) {
    while ((int)((unsigned)__builtin_amdgcn_s_memrealtime() * 256u - t) < 0) __builtin_amdgcn_s_sleep(1);
    SB();
  };
  asm volatile("" :: "s"(x), "s"(kc), "s"(wq), "s"(wg), "s"(wu), "s"(ws), "s"(wd), "s"(wo), "s"(vc));
  const u32x4 xa = *(const u32x4*)(x + 16 * lane), xb = *(const u32x4*)(x + 16 * lane + 8);
  const unsigned xo = *(const unsigned*)(x + 2 * tid);
  unsigned xx = *(const unsigned*)(x + 128 * s + 4 * h + 2 * (lane & 1));
  const auto rw = rsrc(ws, G_END * 8);
  const unsigned epoch = __builtin_amdgcn_raw_buffer_load_b32(rw, 0, 0, 0);
  u32x4 W[4];
  const int rowq = 256 * (s >> 1) + 32 * w + (lane >> 3);
  {
    const auto rq = rsrc(wq, DM * NH * DH * 2);
#pragma unroll
    for (int t = 0; t < 4; ++t) W[t] = bl<AUX_NT>(rq, rowq * (NH * DH * 2) + (2 * h + (s & 1)) * 128 + (lane & 7) * 16, t * 8 * NH * DH * 2);
  }
  SB();
  const char* kh = (const char*)kc + (size_t)h * SK * DH * 2;
  const char* vh = (const char*)vc + (size_t)h * SK * DH * 2;
  const auto rk = rsrc(kh, SK * DH * 2);
  unsigned pfd = 0;
  auto code_prefetch = [&]() {
    u64 pc; int len;
    asm volatile("s_getpc_b64 %0\n.Lpf%=:\n\ts_mov_b32 %1, .Lfdb_end-.Lpf%=" : "=s"(pc), "=s"(len));
    const u32x4 rc = {(unsigned)pc, (unsigned)(pc >> 32) & 0xffffu, (unsigned)max(len, 0), 0x00020000u};
    asm volatile("s_nop 4\n\tbuffer_load_dword %0, %1, %2, 0 offen" : "+v"(pfd) : "v"(tid * 64), "s"(rc));
    SB();
  };
  if (PFA) code_prefetch();
  u32x4 A[4], B[4];
  const unsigned kvo = lane * 16;
#define ISSUE2(rr, so) do { _Pragma("unroll") for (int t = 0; t < 4; ++t) A[t] = bl<AUXK>(rr, kvo + t * 1024, (so)); SB(); \
    _Pragma("unroll") for (int t = 0; t < 4; ++t) B[t] = bl<AUXK>(rr, kvo + t * 1024, (so) + RBYTES); SB(); } while (0)
#define NEXT(so) so += 2 * RBYTES
  const unsigned s0 = s * ROUNDS * RBYTES + w * 4096;
  constexpr int pairs = ROUNDS / 2;
  const unsigned pos = idx;
  unsigned dl = t0 + TK_ * 256u + SLOT / 256 * pos, slot = SLOT;
  auto pace = [&]() {
    until(dl);
    dl += slot;
  };
  if (tid == 0) { sCnt = 0; sCn2 = 0; sCn3 = 0; sQf = 0; }
  {
    float v[16];
#pragma unroll
    for (int k = 0; k < 4; ++k) { v[2 * k] = bf_lo(xa[k]); v[2 * k + 1] = bf_hi(xa[k]); v[8 + 2 * k] = bf_lo(xb[k]); v[9 + 2 * k] = bf_hi(xb[k]); }
    float sum = 0.f;
#pragma unroll
    for (int k = 0; k < 16; ++k) sum += v[k];
    const float mean = wave_sum(sum) * (1.f / DM);
    float sq = 0.f;
#pragma unroll
    for (int k = 0; k < 16; ++k) { const float d = v[k] - mean; sq = fmaf(d, d, sq); }
    const float var = wave_sum(sq) * (1.f / DM);
    const float mb = mrbf(mean), den = mrbf(__builtin_sqrtf(mrbf(mrbf(var) + 1e-5f)));
    *(f32x2*)(sX + 2 * tid) = f32x2{mrbf(mrbf(bf_lo(xo) - mb) / den), mrbf(mrbf(bf_hi(xo) - mb) / den)};
  }
  const unsigned tag = __builtin_amdgcn_readfirstlane(epoch) + 1u;
  asm volatile("" : "+v"(xx));
  SB();
  lds_barrier();
  SB();
  TS(1);
  auto arrive = [&](int* cnt) {
    __builtin_amdgcn_fence(__ATOMIC_RELEASE, "workgroup", "local");
    int old = 0;
    if (lane == 0) old = __hip_atomic_fetch_add(cnt, 1, __ATOMIC_RELAXED, __HIP_MEMORY_SCOPE_WORKGROUP);
    const bool fin = __builtin_amdgcn_readfirstlane(old) == NW - 1;
    __builtin_amdgcn_fence(__ATOMIC_ACQUIRE, "workgroup", "local");
    return fin;
  };
  auto deliver = [&](const f32x2 (&a)[4], float* dst, int* cnt) {
    f32x2 r = xrow_sum8(a);
    r.x += dpp<ROR8>(r.x); r.y += dpp<ROR8>(r.y);
    if (j < 8) *(f32x2*)(dst + w * 64 + 8 * j + 2 * g) = r;
    return arrive(cnt);
  };
  auto qpart = [&]() {
    f32x2 a[4] = {};
    asm volatile("" : "+v"(W[0]), "+v"(W[1]), "+v"(W[2]), "+v"(W[3]));
#pragma unroll
    for (int t = 0; t < 4; ++t) axpy8(a, sX[rowq + 8 * t], W[t]);
    if (deliver(a, sA, &sCnt) && lane < 33) {
      f32x2 t = {};
#pragma unroll
      for (int k = 0; k < NW; ++k) t += *(const f32x2*)(sA + k * 64 + 2 * (lane & 31));
      if (lane == 32) t.x = __uint_as_float(t0);
      g_put2(rw, lane < 32 ? G_Q + b * 64 + 2 * lane : G_T + 2 * b, t, tag);
    }
    SB();
  };
  const int part = h >> 3, rank = h & 7;
  const unsigned col = 7 * s + rank;
  const int rowg = 512 * (part & 1) + 64 * w + (lane >> 3);
  u32x4 X[8];
  unsigned xvo = rowg * (FF * 2) + col * 128 + (lane & 7) * 16;
  asm volatile("" : "+v"(xvo));
  auto xissue = [&]() {
    const auto rx = rsrc(part < 2 ? wg : wu, rank < 7 ? DM * FF * 2 : 0);
    until(t0 + TG_ * 256u + TGW_ * idx);
#pragma unroll
    for (int t = 0; t < 8; ++t) X[t] = bl<AUX_NT>(rx, xvo, t * 8 * FF * 2);
    SB();
  };
  if (w < (int)P6) { xissue(); qpart(); TS(2); } else { qpart(); TS(2); xissue(); }
  {
    f32x2 a[4] = {};
#pragma unroll
    for (int t = 0; t < 8; ++t) axpy8(a, sX[rowg + 8 * t], X[t]);
    if (deliver(a, sA + 512, &sCn2) && lane < 32 && rank < 7) {
      f32x2 t = {};
#pragma unroll
      for (int k = 0; k < NW; ++k) t += *(const f32x2*)(sA + 512 + k * 64 + 2 * lane);
      g_put2(rw, G_H + part * FF + col * 64 + 2 * lane, t, tag);
    }
    SB();
  }
  TS(3);
  u32x4 Wd[4], Wo[4];
  {
    const unsigned od = w < 7 ? (hc * FH + 16 * w + 4 * g) * (DM * 2) + sc * 256 + j * 16 : OOB;
    const unsigned oo = (hc * DH + 16 * w + 4 * g) * (DM * 2) + sc * 256 + j * 16, op = (j & 1) * 2 * (DM * 2);
    const auto rd = rsrc(wd, FF * DM * 2), ro = rsrc(wo, NH * DH * DM * 2);
    until(t0 + TW0_ * 256u + TW1_ * idx);
#pragma unroll
    for (int t = 0; t < 4; ++t) Wd[t] = bl<AUX_NT>(rd, od, t * DM * 2);
#pragma unroll
    for (int t = 0; t < 4; ++t) Wo[t] = bl<AUX_NT>(ro, (oo + (t & 2) * (DM * 2)) ^ op, (t & 1) * DM * 2);
    SB();
  }
  const unsigned oq = w == 0 ? (G_Q + h * 512 + 2 * lane) * 8 : OOB;
  until(dl - TD_ * 256u);
  u32x4 qa = bl<AUX_DEV>(rw, oq, 0), qb = bl<AUX_DEV>(rw, oq, 1024), qc = bl<AUX_DEV>(rw, oq, 2048), qd = bl<AUX_DEV>(rw, oq, 3072);
  const unsigned ot = w == 0 ? (G_T + h * 16 + 2 * (lane & 7)) * 8 : OOB;
  u32x4 qt = bl<AUX_DEV>(rw, ot, 0);
  SB();
  pace(); dl += P7 * 256u;
  ISSUE2(rk, s0);
  const int hp = (j ^ (j >> 2)) & 1;
  const unsigned oh = w < 7 ? (G_H + ((j >> 1) & 3) * FF + hc * FH + 16 * w + 4 * g + 2 * hp) * 8 : OOB;
  u32x4 ha = {};
  if (HP == 0) { ha = bl<AUX_DEV>(rw, oh, 0); SB(); }
  TS(4);
  if (w == 0) {
    SPIN2(qa, oq, qb, oq + 1024, 2);
    SPIN2(qc, oq + 2048, qd, oq + 3072, 2);
    SPIN1(qt, ot, 2);
    int dt = qt[0] - t0;
    int mn = min(max(dt, -400 * 256), 400 * 256), mx = mn;
    mn = min(mn, __builtin_amdgcn_mov_dpp(mn, XOR1, 0xf, 0xf, true)); mn = min(mn, __builtin_amdgcn_mov_dpp(mn, XOR2, 0xf, 0xf, true));
    mn = min(mn, __builtin_amdgcn_mov_dpp(mn, HMIR, 0xf, 0xf, true));
    mx = max(mx, __builtin_amdgcn_mov_dpp(mx, XOR1, 0xf, 0xf, true)); mx = max(mx, __builtin_amdgcn_mov_dpp(mx, XOR2, 0xf, 0xf, true));
    mx = max(mx, __builtin_amdgcn_mov_dpp(mx, HMIR, 0xf, 0xf, true));
    if (lane == 0) sQ[64] = mn + ((mx - mn) * P12 >> 4) ;
    sQ[lane] = cvtpk((__uint_as_float(qa[0]) + __uint_as_float(qb[0])) + (__uint_as_float(qc[0]) + __uint_as_float(qd[0])),
                     (__uint_as_float(qa[2]) + __uint_as_float(qb[2])) + (__uint_as_float(qc[2]) + __uint_as_float(qd[2])));
    __builtin_amdgcn_fence(__ATOMIC_RELEASE, "workgroup", "local");
    if (lane == 0) __hip_atomic_store(&sQf, 1, __ATOMIC_RELAXED, __HIP_MEMORY_SCOPE_WORKGROUP);
  }
  while (__hip_atomic_load(&sQf, __ATOMIC_RELAXED, __HIP_MEMORY_SCOPE_WORKGROUP) == 0) __builtin_amdgcn_s_sleep(1);
  __builtin_amdgcn_fence(__ATOMIC_ACQUIRE, "workgroup", "local");
  u32x4 q = *(const u32x4*)(sQ + 4 * j);
  const unsigned shift = __builtin_amdgcn_readfirstlane(sQ[64]);
  dl += shift;
  const unsigned tS = t0 + shift + (TK_ + P7 + PY1 + PY2) * 256u + 64 * SLOT;
  asm volatile("" : "+v"(q));
  TS(5);
  SB();
  float m = -1e30f;
  float* st = sT + w * (ROUNDS * 16) + 4 * g;
  auto dots = [&](const u32x4 (&KK)[4], float (&d)[4]) {
#pragma unroll
    for (int t = 0; t < 4; ++t) d[t] = dot2(KK[t][3], q[3], dot2(KK[t][2], q[2], dot2(KK[t][1], q[1], dot2(KK[t][0], q[0], 0.f))));
    asm volatile("" : "+v"(d[0]), "+v"(d[1]), "+v"(d[2]), "+v"(d[3]));
    SB();
  };
  auto score = [&](const float (&d)[4], float* p) {
    const float y0 = d[0] + dpp<ROR8>(d[0]), y1 = d[1] + dpp<ROR8>(d[1]), y2 = d[2] + dpp<ROR8>(d[2]), y3 = d[3] + dpp<ROR8>(d[3]);
    const float ya = (j & 8) ? y2 : y0, yb = (j & 8) ? y3 : y1;
    const float za = ya + dpp<HMIR>(ya), zb = yb + dpp<HMIR>(yb);
    float sc = (j & 4) ? zb : za;
    sc += dpp<XOR1>(sc); sc += dpp<XOR2>(sc);
    const float t2 = MIMIC ? rbf(rbf(sc) * SCALE) * LOG2E : sc * (SCALE * LOG2E);
    m = fmaxf(m, t2);
    if ((j & 3) == 0) p[j >> 2] = t2;
    SB();
  };
  {
    unsigned so = s0;
    float* p = st;
    float d[4], e[4];
    auto kit = [&](bool gather) {
      NEXT(so);
      dots(A, d); dots(B, e);
      pace();
      ISSUE2(rk, so);
      if (gather) { ha = bl<AUX_DEV>(rw, oh, 0); SB(); }
      score(d, p); score(e, p + 16);
      p += 32;
    };
    if (HP == 1) kit(true);
    kit(false);
#pragma unroll 1
    for (int it = 2 + HP; it < pairs; ++it) { kit(false); if (PFB && it == (int)PFB) code_prefetch(); }
    dots(A, d); dots(B, e);
    dl += PY1 * 256u;
    pace();
    dl += PY2 * 256u;
    const auto rv = rsrc(vh, SK * DH * 2);
    ISSUE2(rv, s0);
    score(d, p); score(e, p + 16);
    TS(10);
  }
  auto quad4 = [&](float a0, float a1, bool up, float (&r)[4]) {
    const float b0 = dpp<XOR1>(a0), b1 = dpp<XOR1>(a1);
    r[0] = up ? b0 : a0; r[1] = up ? b1 : a1; r[2] = up ? a0 : b0; r[3] = up ? a1 : b1;
  };
  m = fmaxf(m, dpp<ROR4>(m)); m = xrow_max(fmaxf(m, dpp<ROR8>(m)));
  __builtin_amdgcn_wave_barrier();
  SB();
  {
    float l = 0.f;
    float* p = sT + w * (ROUNDS * 16) + 4 * lane;
#pragma unroll
    for (int k = 0; k < ROUNDS / 16; ++k) {
      f32x4 e = *(const f32x4*)(p + 256 * k);
#pragma unroll
      for (int t = 0; t < 4; ++t) e[t] = __builtin_amdgcn_exp2f(e[t] - m);
      *(f32x4*)(p + 256 * k) = e;
      l += (e[0] + e[1]) + (e[2] + e[3]);
    }
    l = wave_sum(l);
    if (lane == 0) *(f32x2*)(sM + 2 * w) = f32x2{m, l};
    if (arrive(&sCn3)) {
      const f32x2 r = merge8(*(const f32x2*)(sM + 2 * (lane & 7)));
      if (lane == 0) g_put2(rw, G_M + 2 * b, r, tag);
    }
    SB();
  }
  f32x2 z[4] = {};
  {
    if (w < 7) SPIN1(ha, oh, 2);
    float v0 = __uint_as_float(ha[0]), v1 = __uint_as_float(ha[2]);
    v0 = mrbf(v0 + dpp<XOR2>(v0)); v1 = mrbf(v1 + dpp<XOR2>(v1));
    const float u0 = dpp<HMIR>(v0), u1 = dpp<HMIR>(v1);
    auto glu = [&](float own, float other) { return mrbf(mrbf(1.f / (1.f + __expf(-((j & 4) ? other : own)))) * ((j & 4) ? own : other)); };
    float hv[4];
    quad4(glu(v0, u0), glu(v1, u1), hp, hv);
#pragma unroll
    for (int t = 0; t < 4; ++t) axpy8(z, hv[t], Wd[t]);
#pragma unroll
    for (int t = 0; t < 4; t += 2)
#pragma unroll
      for (int k = 0; k < 4; ++k) {
        const unsigned r0 = Wo[t][k], r1 = Wo[t + 1][k];
        Wo[t][k] = __builtin_amdgcn_perm(r1, r0, 0x05040100); Wo[t + 1][k] = __builtin_amdgcn_perm(r1, r0, 0x07060302);
      }
    asm volatile("" : "+v"(z[0]), "+v"(z[1]), "+v"(z[2]), "+v"(z[3]), "+v"(Wo[0]), "+v"(Wo[1]), "+v"(Wo[2]), "+v"(Wo[3]));
    SB();
  }
  f32x2 acc[4] = {};
  float f, fk[NW];
  {
    unsigned so = s0;
    const auto rv = rsrc(vh, SK * DH * 2);
    const unsigned om = w == 0 ? (G_M + h * 16 + 2 * (lane & 7)) * 8 : OOB;
    u32x4 gm = {};
    auto pv = [&](const float* p, const u32x4 (&VV)[4]) {
      const f32x4 e = *(const f32x4*)p;
#pragma unroll
      for (int t = 0; t < 4; ++t) axpy8(acc, e[t], VV[t]);
      SB();
    };
    auto vit = [&](bool gather) {
      NEXT(so);
      pv(st, A);
      pv(st + 16, B);
      asm volatile("" : "+v"(acc[0]), "+v"(acc[1]), "+v"(acc[2]), "+v"(acc[3]));
      pace();
      ISSUE2(rv, so);
      if (gather) { gm = bl<AUX_DEV>(rw, om, 0); SB(); }
      st += 32;
    };
#pragma unroll 1
    for (int it = 1; it < GE; ++it) { vit(false); if (PFC && it == (int)PFC) code_prefetch(); }
    vit(true); vit(false);
#pragma unroll 1
    for (int it = GE + 2; it < pairs; ++it) vit(false);
    if (w == 0) {
      SPIN1(gm, om, 2);
      const f32x2 r = merge8(f32x2{__uint_as_float(gm[0]), __uint_as_float(gm[2])});
      while (__hip_atomic_load(&sCn3, __ATOMIC_RELAXED, __HIP_MEMORY_SCOPE_WORKGROUP) < NW) __builtin_amdgcn_s_sleep(1);
      __builtin_amdgcn_fence(__ATOMIC_ACQUIRE, "workgroup", "local");
      f = __builtin_amdgcn_exp2f(sM[2 * (lane & 7)] - r.x) / r.y;
    } else f = 0.f;
#pragma unroll
    for (int k = 0; k < NW; ++k) { fk[k] = __builtin_bit_cast(float, __builtin_amdgcn_readlane(__builtin_bit_cast(int, f), k)); asm volatile("" : "+s"(fk[k])); }
    SB();
    pv(st, A);
    pv(st + 16, B);
  }
  asm volatile("" :: "v"(pfd));
  TS(6);
  SB();
  *(f32x2*)(sB + w * 128 + 8 * j + 2 * g) = xrow_sum8(acc);
  SB();
  lds_barrier();
  unsigned tB = (unsigned)__builtin_amdgcn_s_memrealtime() * 256u;
  TS(7);
  auto sum8 = [&](const float* p) {
    f32x2 t[NW];
#pragma unroll
    for (int k = 0; k < NW; ++k) t[k] = *(const f32x2*)(p + k * 128 + 2 * lane);
    return ((t[0] + t[1]) + (t[2] + t[3])) + ((t[4] + t[5]) + (t[6] + t[7]));
  };
  if (w == 0) {
    f32x2 t[NW];
#pragma unroll
    for (int k = 0; k < NW; ++k) t[k] = *(const f32x2*)(sB + k * 128 + 2 * lane);
    f32x2 u = t[0] * fk[0], v = t[1] * fk[1];
#pragma unroll
    for (int k = 2; k < NW; k += 2) { u = __builtin_elementwise_fma(t[k], f32x2{fk[k], fk[k]}, u); v = __builtin_elementwise_fma(t[k + 1], f32x2{fk[k + 1], fk[k + 1]}, v); }
    g_put2(rw, G_1 + b * 128 + 2 * lane, u + v, tag);
  }
  SB();
  const unsigned o1 = (G_1 + (hc * NS + (j >> 1)) * 128 + 16 * w + 4 * g + 2 * (j & 1)) * 8;
  until(tB + 36 * 256u);
  until(tS + P8 * 256u);
  u32x4 pa = bl<AUXP>(rw, o1, 0);
  SB();
  SPIN1(pa, o1, 4);
  TS(8);
  {
    const unsigned p0 = cvtpk(sum_p(__uint_as_float(pa[0])), sum_p(__uint_as_float(pa[2])));
    const unsigned p1 = __builtin_amdgcn_mov_dpp(p0, XOR1, 0xf, 0xf, true);
#pragma unroll
    for (int k = 0; k < 4; ++k) { z[k].x = dot2(Wo[0][k], p0, z[k].x); z[k].y = dot2(Wo[1][k], p0, z[k].y); }
#pragma unroll
    for (int k = 0; k < 4; ++k) { z[k].x = dot2(Wo[2][k], p1, z[k].x); z[k].y = dot2(Wo[3][k], p1, z[k].y); }
    *(f32x2*)(sA + w * 128 + 8 * j + 2 * g) = xrow_sum8(z);
  }
  lds_barrier();
  if (w == 0) g_put2(rw, G_2 + (sc * NH + hc) * 128 + 2 * lane, sum8(sA), tag);
  TS(9);
  if (w == 2) {
    const unsigned o2 = (G_2 + (s * NH + (lane >> 1)) * 128 + 4 * h + 2 * (lane & 1)) * 8;
    tB = (unsigned)__builtin_amdgcn_s_memrealtime() * 256u;
    until(tB + 28 * 256u);
    until(tS + P9 * 256u);
    u32x4 fa = bl<AUXP>(rw, o2, 0);
    SPIN1(fa, o2, 1);
    float v0 = __uint_as_float(fa[0]), v1 = __uint_as_float(fa[2]);
    v0 = xrow_sum(sum_p(v0)); v1 = xrow_sum(sum_p(v1));
    if (lane < 2) out[64 * s + 2 * h + lane] = cvtpk(bf_lo(xx) + v0, bf_hi(xx) + v1);
    if (b == 0 && lane == 0) __hip_atomic_store((unsigned*)ws, tag, __ATOMIC_RELAXED, __HIP_MEMORY_SCOPE_AGENT);
  }
  asm volatile(".Lfdb_end:");
  TS(11);
}

extern "C" void kernel_launch(void* const* d_in, const int* in_sizes, int n_in, void* d_out, int out_size, void* d_ws,
                              size_t ws_size, hipStream_t stream) {
  (void)in_sizes; (void)n_in; (void)out_size; (void)ws_size;
  fused_decode_block<<<NWG, NT, 0, stream>>>((const unsigned short*)d_in[0], d_in[1], d_in[2], d_in[3], d_in[4], d_in[6],
                                              d_in[7], d_in[8], (unsigned*)d_out, (u64*)((char*)d_ws + WSOFF) XVALS);
}
```

```cpp
#include <hip/hip_runtime.h>
#include <hip/hip_bf16.h>
#include <stdint.h>

#define MIMIC 1
#define PROF 0

typedef unsigned u32x4 __attribute__((ext_vector_type(4)));
typedef unsigned u32x2 __attribute__((ext_vector_type(2)));
typedef float f32x2 __attribute__((ext_vector_type(2)));
typedef float f32x4 __attribute__((ext_vector_type(4)));
typedef __bf16 bf16x2 __attribute__((ext_vector_type(2)));
typedef unsigned long long u64;
typedef u64 u64x2 __attribute__((ext_vector_type(2)));
typedef u64 u64x8 __attribute__((ext_vector_type(8)));
#define GLOBAL __attribute__((address_space(1)))

constexpr int DM = 1024, NH = 32, SK = 65536, DH = 128, FF = 3584;
constexpr int NS = 8;
constexpr int NW = 8;
constexpr int NT = NW * 64;
constexpr int NWG = NH * NS;
constexpr int FH = FF / NH;
constexpr int ROUNDS = SK / NS / 16 / NW;
constexpr unsigned RBYTES = NW * 4096;
constexpr unsigned OOB = 0x80000000u;
#define AUXK 2
#define AUXV 2
#define AUXP 16
constexpr int AUX_NT = 2, AUX_DEV = 16;
constexpr float LOG2E = 1.4426950408889634f;
constexpr float SCALE = 0.08838834764831845f;
constexpr int SPIN_MAX = 1 << 18;

#define DD 13800
#define TG 150
#define TGW 200
#define TK 620
#define TW0 370
#define TW1 200
#define WSOFF 0
#define TD 80
#define WS 4
#define WE 3
#define TX 0
#define TP1 116
#define TP2 214
#define RM 1
#define TA 12
#define TH 0
#define SG 0
#ifndef CU_
#define CU_ 0
#endif
#define HS 0
#define TY1 0
#define TY2 0
#define PF0 1
#define PF1 0
#define PF2 0
#define LT0 0
#define CT0 1
#define WARM 0
#define ORDER 0
#define HP 1
#define GE 30
constexpr unsigned SLOT = DD * 256u / 64, TK_ = TK, TG_ = TG, TW0_ = TW0, TW1_ = TW1, TGW_ = TGW, TD_ = TD, P6 = WE, P7 = TX, P8 = TP1, P9 = TP2, P10 = RM, P11 = TA, P12 = TH, P13 = SG, P14 = CU_, P15 = HS;
#define XARGS
#define XVALS
#define PFA PF0
#define PFB PF1
#define PFC PF2
#define PY1 TY1
#define PY2 TY2
constexpr size_t G_Q = 32;
constexpr size_t G_H = G_Q + NH * 4 * DH;
constexpr size_t G_T = G_H + 4 * FF;
constexpr size_t G_M = G_T + NWG * 2;
constexpr size_t G_1 = G_M + NWG * 2;
constexpr size_t G_2 = G_1 + (size_t)NWG * 128;
constexpr size_t G_END = G_2 + (size_t)NWG * 128;
constexpr size_t WS_PROF = (G_END * 8 + 4095) / 4096 * 4096;

__device__ __forceinline__ float bf_lo(unsigned u) { return __builtin_bit_cast(float, u << 16); }
__device__ __forceinline__ float bf_hi(unsigned u) { return __builtin_bit_cast(float, u & 0xffff0000u); }
__device__ __forceinline__ unsigned cvtpk(float lo, float hi) {
  bf16x2 r = {(__bf16)lo, (__bf16)hi};
  return __builtin_bit_cast(unsigned, r);
}
__device__ __forceinline__ float rbf(float x) { return bf_lo(cvtpk(x, 0.f)); }
__device__ __forceinline__ float mrbf(float x) { return MIMIC ? rbf(x) : x; }
__device__ __forceinline__ float dot2(unsigned a, unsigned b, float c) {
  return __builtin_amdgcn_fdot2_f32_bf16(__builtin_bit_cast(bf16x2, a), __builtin_bit_cast(bf16x2, b), c, false);
}
template <int CTRL> __device__ __forceinline__ float dpp(float x) {
  return __builtin_bit_cast(float, __builtin_amdgcn_mov_dpp(__builtin_bit_cast(int, x), CTRL, 0xf, 0xf, true));
}
constexpr int XOR1 = 0xB1, XOR2 = 0x4E, HMIR = 0x141, ROR8 = 0x128, ROR4 = 0x124, ROR2 = 0x122, ROR1 = 0x121, BCAST = 0x150;
__device__ __forceinline__ float xrow_sum(float x) {
  auto s = __builtin_amdgcn_permlane16_swap(__float_as_uint(x), __float_as_uint(x), false, false);
  x = __uint_as_float(s[0]) + __uint_as_float(s[1]);
  auto t = __builtin_amdgcn_permlane32_swap(__float_as_uint(x), __float_as_uint(x), false, false);
  return __uint_as_float(t[0]) + __uint_as_float(t[1]);
}
__device__ __forceinline__ float xrow_max(float x) {
  auto s = __builtin_amdgcn_permlane16_swap(__float_as_uint(x), __float_as_uint(x), false, false);
  x = fmaxf(__uint_as_float(s[0]), __uint_as_float(s[1]));
  auto t = __builtin_amdgcn_permlane32_swap(__float_as_uint(x), __float_as_uint(x), false, false);
  return fmaxf(__uint_as_float(t[0]), __uint_as_float(t[1]));
}
__device__ __forceinline__ float wave_sum(float x) {
  x += dpp<ROR8>(x); x += dpp<ROR4>(x); x += dpp<ROR2>(x); x += dpp<ROR1>(x);
  return xrow_sum(x);
}
__device__ __forceinline__ void lds_barrier() {
  __builtin_amdgcn_fence(__ATOMIC_RELEASE, "workgroup", "local");
  __builtin_amdgcn_s_barrier();
  __builtin_amdgcn_fence(__ATOMIC_ACQUIRE, "workgroup", "local");
}
__device__ __forceinline__ void g_put(u64* p, float v, unsigned tag) {
  __hip_atomic_store(p, ((u64)tag << 32) | __float_as_uint(v), __ATOMIC_RELAXED, __HIP_MEMORY_SCOPE_AGENT);
}
__device__ __forceinline__ void g_put2(__amdgpu_buffer_rsrc_t r, unsigned granule, unsigned a, unsigned b, unsigned tag) {
  __builtin_amdgcn_raw_buffer_store_b128(u32x4{a, tag, b, tag}, r, granule * 8, 0, AUX_DEV);
}
__device__ __forceinline__ void g_put2(__amdgpu_buffer_rsrc_t r, unsigned granule, f32x2 v, unsigned tag) {
  g_put2(r, granule, __float_as_uint(v.x), __float_as_uint(v.y), tag);
}
__device__ __forceinline__ bool g_ok(u32x4 g, unsigned tag) { return g[1] == tag && g[3] == tag; }
__device__ __forceinline__ __amdgpu_buffer_rsrc_t rsrc(const void* p, unsigned bytes) {
  return __builtin_amdgcn_make_buffer_rsrc((void*)p, 0, (int)bytes, 0x00020000);
}
template <int AUX> __device__ __forceinline__ u32x4 bl(__amdgpu_buffer_rsrc_t r, unsigned voff, unsigned soff) {
  return __builtin_amdgcn_raw_buffer_load_b128(r, voff, soff, AUX);
}
__device__ __forceinline__ void axpy8(f32x2 (&acc)[4], float a, u32x4 w) {
  const f32x2 aa = {a, a};
#pragma unroll
  for (int k = 0; k < 4; ++k) { const f32x2 v = {bf_lo(w[k]), bf_hi(w[k])}; acc[k] = __builtin_elementwise_fma(v, aa, acc[k]); }
}
__device__ __forceinline__ f32x2 xrow_sum8(const f32x2 (&a)[4]) {
  auto h32 = [](float p, float q) {
    auto s = __builtin_amdgcn_permlane32_swap(__float_as_uint(p), __float_as_uint(q), false, false);
    return __uint_as_float(s[0]) + __uint_as_float(s[1]); };
  auto h16 = [](float p, float q) {
    auto s = __builtin_amdgcn_permlane16_swap(__float_as_uint(p), __float_as_uint(q), false, false);
    return __uint_as_float(s[0]) + __uint_as_float(s[1]); };
  return f32x2{h16(h32(a[0].x, a[2].x), h32(a[1].x, a[3].x)), h16(h32(a[0].y, a[2].y), h32(a[1].y, a[3].y))};
}
__device__ __forceinline__ float sum_p(float x) { x += dpp<ROR2>(x); x += dpp<ROR4>(x); return x + dpp<ROR8>(x); }
__device__ __forceinline__ f32x2 merge8(f32x2 v) {
  float M = fmaxf(v.x, dpp<XOR1>(v.x)); M = fmaxf(M, dpp<XOR2>(M)); M = fmaxf(M, dpp<HMIR>(M));
  float L = v.y * __builtin_amdgcn_exp2f(v.x - M);
  L += dpp<XOR1>(L); L += dpp<XOR2>(L); L += dpp<HMIR>(L);
  return f32x2{M, L};
}
#define SB() __builtin_amdgcn_sched_barrier(0)
#define VMWAIT(n) do { __builtin_amdgcn_s_waitcnt(0xF70 | (n)); SB(); } while (0)
#define SPIN2(a, oa, c, oc, slp) do { if (!__all(g_ok(a, tag) && g_ok(c, tag))) { _Pragma("unroll 1") for (int it = 0; it < SPIN_MAX; ++it) { \
    asm volatile("" ::: "memory"); a = bl<AUX_DEV>(rw, oa, 0); c = bl<AUX_DEV>(rw, oc, 0); \
    if (__all(g_ok(a, tag) && g_ok(c, tag))) break; __builtin_amdgcn_s_sleep(slp); } } } while (0)
#define SPIN1(a, oa, slp) do { if (!__all(g_ok(a, tag))) { _Pragma("unroll 1") for (int it = 0; it < SPIN_MAX; ++it) { \
    asm volatile("" ::: "memory"); a = bl<AUXP>(rw, oa, 0); \
    if (__all(g_ok(a, tag))) break; __builtin_amdgcn_s_sleep(slp); } } } while (0)
#define TS(i) do {} while (0)

__global__ void __launch_bounds__(NT, 2)
fused_decode_block(const void*, const void*, const void*, const void*, const void*, const void*, const void*, const void*, void*, void* XARGS) {
  __shared__ __attribute__((aligned(16))) float sA[NW * 128];
  __shared__ __attribute__((aligned(16))) float sB[NW * 128];
  __shared__ int sCnt, sCn2, sCn3, sQf;
  __shared__ __attribute__((aligned(16))) unsigned sQ[68];
  __shared__ __attribute__((aligned(16))) float sX[DM];
  __shared__ __attribute__((aligned(16))) float sM[2 * NW];
  __shared__ __attribute__((aligned(16))) float sT[NW * ROUNDS * 16];
  const int tid = threadIdx.x, lane = tid & 63, g = lane >> 4, j = lane & 15;
  const int w = __builtin_amdgcn_readfirstlane(tid >> 6);
  const int b = blockIdx.x, s = b & 7, h = b >> 3;
  const int hc = P10 ? 4 * s + (h & 3) : h, sc = P10 ? h >> 2 : s;
  unsigned pfd;
  u64x8 ka; u64x2 kb;
  {
    u64 pc;
    asm volatile("s_load_dwordx16 %2, %4, 0x0\n\ts_load_dwordx4 %3, %4, 0x40\n\ts_getpc_b64 %1\n.Lpf%=:\n\tv_mul_u32_u24 %0, %6, %5\n\t"
                 "v_min_u32 %0, .Lfdb_end-.Lpf%=, %0\n\tglobal_load_dword %0, %0, %1\n\ts_waitcnt lgkmcnt(0)"
                 : "=&v"(pfd), "=&s"(pc), "=&s"(ka), "=&s"(kb) : "s"(__builtin_amdgcn_kernarg_segment_ptr()), "v"(tid), "s"(PFA && !(h & PFB) ? 64 : 0));
  }
  SB();
  const unsigned short* x = (const unsigned short*)(const GLOBAL unsigned short*)ka[0];
  const void *kc = (const void*)(const GLOBAL char*)ka[1], *vc = (const void*)(const GLOBAL char*)ka[2], *wq = (const void*)(const GLOBAL char*)ka[3],
             *wo = (const void*)(const GLOBAL char*)ka[4], *wg = (const void*)(const GLOBAL char*)ka[5], *wu = (const void*)(const GLOBAL char*)ka[6],
             *wd = (const void*)(const GLOBAL char*)ka[7];
  unsigned* out = (unsigned*)(GLOBAL unsigned*)kb[0];
  u64* ws = (u64*)(GLOBAL u64*)kb[1];
  TS(0);
  unsigned t0 = (unsigned)__builtin_amdgcn_s_memrealtime() * 256u;
  const unsigned idx = w * 32 + (h & 3) * 8 + s;
  auto until = [](unsigned t) {
    while ((int)((unsigned)__builtin_amdgcn_s_memrealtime() * 256u - t) < 0) __builtin_amdgcn_s_sleep(1);
    SB();
  };
  const u32x4 xa = *(const u32x4*)(x + 16 * lane), xb = *(const u32x4*)(x + 16 * lane + 8);
  const unsigned xo = *(const unsigned*)(x + 2 * tid);
  unsigned xx = *(const unsigned*)(x + 128 * s + 4 * h + 2 * (lane & 1));
  const auto rw = rsrc(ws, G_END * 8);
  const unsigned epoch = __builtin_amdgcn_raw_buffer_load_b32(rw, 0, 0, 0);
  u32x4 W[4];
  const int rowq = 256 * (s >> 1) + 32 * w + (lane >> 3);
  {
    const auto rq = rsrc(wq, DM * NH * DH * 2);
#pragma unroll
    for (int t = 0; t < 4; ++t) W[t] = bl<AUX_NT>(rq, rowq * (NH * DH * 2) + (2 * h + (s & 1)) * 128 + (lane & 7) * 16, t * 8 * NH * DH * 2);
  }
  SB();
  const char* kh = (const char*)kc + (size_t)h * SK * DH * 2;
  const char* vh = (const char*)vc + (size_t)h * SK * DH * 2;
  const auto rk = rsrc(kh, SK * DH * 2);
  u32x4 A[4], B[4];
  const unsigned kvo = lane * 16;
#define ISSUE2(rr, so) do { _Pragma("unroll") for (int t = 0; t < 4; ++t) A[t] = bl<AUXK>(rr, kvo + t * 1024, (so)); SB(); \
    _Pragma("unroll") for (int t = 0; t < 4; ++t) B[t] = bl<AUXK>(rr, kvo + t * 1024, (so) + RBYTES); SB(); } while (0)
#define NEXT(so) so += 2 * RBYTES
  const unsigned s0 = s * ROUNDS * RBYTES + w * 4096;
  constexpr int pairs = ROUNDS / 2;
  const unsigned pos = idx;
  unsigned dl, slot = SLOT;
  auto pace = [&]() {
    until(dl);
    dl += slot;
  };
  if (tid == 0) { sCnt = 0; sCn2 = 0; sCn3 = 0; sQf = 0; sQ[65] = t0; }
  {
    float v[16];
#pragma unroll
    for (int k = 0; k < 4; ++k) { v[2 * k] = bf_lo(xa[k]); v[2 * k + 1] = bf_hi(xa[k]); v[8 + 2 * k] = bf_lo(xb[k]); v[9 + 2 * k] = bf_hi(xb[k]); }
    float sum = 0.f;
#pragma unroll
    for (int k = 0; k < 16; ++k) sum += v[k];
    const float mean = wave_sum(sum) * (1.f / DM);
    float sq = 0.f;
#pragma unroll
    for (int k = 0; k < 16; ++k) { const float d = v[k] - mean; sq = fmaf(d, d, sq); }
    const float var = wave_sum(sq) * (1.f / DM);
    const float mb = mrbf(mean), den = mrbf(__builtin_sqrtf(mrbf(mrbf(var) + 1e-5f)));
    *(f32x2*)(sX + 2 * tid) = f32x2{mrbf(mrbf(bf_lo(xo) - mb) / den), mrbf(mrbf(bf_hi(xo) - mb) / den)};
  }
  const unsigned tag = __builtin_amdgcn_readfirstlane(epoch) + 1u;
  asm volatile("" : "+v"(xx));
  SB();
  lds_barrier();
  SB();
  if (CT0) t0 = __builtin_amdgcn_readfirstlane(sQ[65]);
  dl = t0 + TK_ * 256u + SLOT / 256 * pos;
  TS(1);
  auto arrive = [&](int* cnt) {
    __builtin_amdgcn_fence(__ATOMIC_RELEASE, "workgroup", "local");
    int old = 0;
    if (lane == 0) old = __hip_atomic_fetch_add(cnt, 1, __ATOMIC_RELAXED, __HIP_MEMORY_SCOPE_WORKGROUP);
    const bool fin = __builtin_amdgcn_readfirstlane(old) == NW - 1;
    __builtin_amdgcn_fence(__ATOMIC_ACQUIRE, "workgroup", "local");
    return fin;
  };
  auto deliver = [&](const f32x2 (&a)[4], float* dst, int* cnt) {
    f32x2 r = xrow_sum8(a);
    r.x += dpp<ROR8>(r.x); r.y += dpp<ROR8>(r.y);
    if (j < 8) *(f32x2*)(dst + w * 64 + 8 * j + 2 * g) = r;
    return arrive(cnt);
  };
  auto qpart = [&]() {
    f32x2 a[4] = {};
    asm volatile("" : "+v"(W[0]), "+v"(W[1]), "+v"(W[2]), "+v"(W[3]));
#pragma unroll
    for (int t = 0; t < 4; ++t) axpy8(a, sX[rowq + 8 * t], W[t]);
    if (deliver(a, sA, &sCnt) && lane < 33) {
      f32x2 t = {};
#pragma unroll
      for (int k = 0; k < NW; ++k) t += *(const f32x2*)(sA + k * 64 + 2 * (lane & 31));
      if (lane == 32) t.x = __uint_as_float(t0);
      g_put2(rw, lane < 32 ? G_Q + b * 64 + 2 * lane : G_T + 2 * b, t, tag);
    }
    SB();
  };
  const int part = h >> 3, rank = h & 7;
  const unsigned col = 7 * s + rank;
  const int rowg = 512 * (part & 1) + 64 * w + (lane >> 3);
  u32x4 X[8];
  unsigned xvo = rowg * (FF * 2) + col * 128 + (lane & 7) * 16;
  asm volatile("" : "+v"(xvo));
  auto xissue = [&]() {
    const auto rx = rsrc(part < 2 ? wg : wu, rank < 7 ? DM * FF * 2 : 0);
    until(t0 + TG_ * 256u + TGW_ * idx);
#pragma unroll
    for (int t = 0; t < 8; ++t) X[t] = bl<AUX_NT>(rx, xvo, t * 8 * FF * 2);
    SB();
  };
  if (w < (int)P6) { xissue(); qpart(); TS(2); } else { qpart(); TS(2); xissue(); }
  {
    f32x2 a[4] = {};
#pragma unroll
    for (int t = 0; t < 8; ++t) axpy8(a, sX[rowg + 8 * t], X[t]);
    if (deliver(a, sA + 512, &sCn2) && lane < 32 && rank < 7) {
      f32x2 t = {};
#pragma unroll
      for (int k = 0; k < NW; ++k) t += *(const f32x2*)(sA + 512 + k * 64 + 2 * lane);
      g_put2(rw, G_H + part * FF + col * 64 + 2 * lane, t, tag);
    }
    SB();
  }
  TS(3);
  u32x4 Wd[4], Wo[4];
  {
    const unsigned od = w < 7 ? (hc * FH + 16 * w + 4 * g) * (DM * 2) + sc * 256 + j * 16 : OOB;
    const unsigned oo = (hc * DH + 16 * w + 4 * g) * (DM * 2) + sc * 256 + j * 16, op = (j & 1) * 2 * (DM * 2);
    const auto rd = rsrc(wd, FF * DM * 2), ro = rsrc(wo, NH * DH * DM * 2);
    until(t0 + TW0_ * 256u + TW1_ * idx);
#pragma unroll
    for (int t = 0; t < 4; ++t) Wd[t] = bl<AUX_NT>(rd, od, t * DM * 2);
#pragma unroll
    for (int t = 0; t < 4; ++t) Wo[t] = bl<AUX_NT>(ro, (oo + (t & 2) * (DM * 2)) ^ op, (t & 1) * DM * 2);
    SB();
  }
  const unsigned oq = w == 0 ? (G_Q + h * 512 + 2 * lane) * 8 : OOB;
  until(dl - TD_ * 256u);
  u32x4 qa = bl<AUX_DEV>(rw, oq, 0), qb = bl<AUX_DEV>(rw, oq, 1024), qc = bl<AUX_DEV>(rw, oq, 2048), qd = bl<AUX_DEV>(rw, oq, 3072);
  const unsigned ot = w == 0 ? (G_T + h * 16 + 2 * (lane & 7)) * 8 : OOB;
  u32x4 qt = bl<AUX_DEV>(rw, ot, 0);
  SB();
  pace(); dl += P7 * 256u;
  ISSUE2(rk, s0);
  const int hp = (j ^ (j >> 2)) & 1;
  const unsigned oh = w < 7 ? (G_H + ((j >> 1) & 3) * FF + hc * FH + 16 * w + 4 * g + 2 * hp) * 8 : OOB;
  u32x4 ha = {};
  if (HP == 0) { ha = bl<AUX_DEV>(rw, oh, 0); SB(); }
  TS(4);
  if (w == 0) {
    SPIN2(qa, oq, qb, oq + 1024, 2);
    SPIN2(qc, oq + 2048, qd, oq + 3072, 2);
    SPIN1(qt, ot, 2);
    int dt = qt[0] - t0;
    int mn = min(max(dt, -400 * 256), 400 * 256), mx = mn;
    mn = min(mn, __builtin_amdgcn_mov_dpp(mn, XOR1, 0xf, 0xf, true)); mn = min(mn, __builtin_amdgcn_mov_dpp(mn, XOR2, 0xf, 0xf, true));
    mn = min(mn, __builtin_amdgcn_mov_dpp(mn, HMIR, 0xf, 0xf, true));
    mx = max(mx, __builtin_amdgcn_mov_dpp(mx, XOR1, 0xf, 0xf, true)); mx = max(mx, __builtin_amdgcn_mov_dpp(mx, XOR2, 0xf, 0xf, true));
    mx = max(mx, __builtin_amdgcn_mov_dpp(mx, HMIR, 0xf, 0xf, true));
    if (lane == 0) sQ[64] = mn + ((mx - mn) * P12 >> 4) ;
    sQ[lane] = cvtpk((__uint_as_float(qa[0]) + __uint_as_float(qb[0])) + (__uint_as_float(qc[0]) + __uint_as_float(qd[0])),
                     (__uint_as_float(qa[2]) + __uint_as_float(qb[2])) + (__uint_as_float(qc[2]) + __uint_as_float(qd[2])));
    __builtin_amdgcn_fence(__ATOMIC_RELEASE, "workgroup", "local");
    if (lane == 0) __hip_atomic_store(&sQf, 1, __ATOMIC_RELAXED, __HIP_MEMORY_SCOPE_WORKGROUP);
  }
  while (__hip_atomic_load(&sQf, __ATOMIC_RELAXED, __HIP_MEMORY_SCOPE_WORKGROUP) == 0) __builtin_amdgcn_s_sleep(1);
  __builtin_amdgcn_fence(__ATOMIC_ACQUIRE, "workgroup", "local");
  u32x4 q = *(const u32x4*)(sQ + 4 * j);
  const unsigned shift = __builtin_amdgcn_readfirstlane(sQ[64]);
  dl += shift;
  const unsigned tS = t0 + shift + (TK_ + P7 + PY1 + PY2) * 256u + 64 * SLOT;
  asm volatile("" : "+v"(q));
  TS(5);
  SB();
  float m = -1e30f;
  float* st = sT + w * (ROUNDS * 16) + 4 * g;
  auto dots = [&](const u32x4 (&KK)[4], float (&d)[4]) {
#pragma unroll
    for (int t = 0; t < 4; ++t) d[t] = dot2(KK[t][3], q[3], dot2(KK[t][2], q[2], dot2(KK[t][1], q[1], dot2(KK[t][0], q[0], 0.f))));
    asm volatile("" : "+v"(d[0]), "+v"(d[1]), "+v"(d[2]), "+v"(d[3]));
    SB();
  };
  auto score = [&](const float (&d)[4], float* p) {
    const float y0 = d[0] + dpp<ROR8>(d[0]), y1 = d[1] + dpp<ROR8>(d[1]), y2 = d[2] + dpp<ROR8>(d[2]), y3 = d[3] + dpp<ROR8>(d[3]);
    const float ya = (j & 8) ? y2 : y0, yb = (j & 8) ? y3 : y1;
    const float za = ya + dpp<HMIR>(ya), zb = yb + dpp<HMIR>(yb);
    float sc = (j & 4) ? zb : za;
    sc += dpp<XOR1>(sc); sc += dpp<XOR2>(sc);
    const float t2 = MIMIC ? rbf(rbf(sc) * SCALE) * LOG2E : sc * (SCALE * LOG2E);
    m = fmaxf(m, t2);
    if ((j & 3) == 0) p[j >> 2] = t2;
    SB();
  };
  {
    unsigned so = s0;
    float* p = st;
    float d[4], e[4];
    auto kit = [&](bool gather) {
      NEXT(so);
      dots(A, d); dots(B, e);
      pace();
      ISSUE2(rk, so);
      if (gather) { ha = bl<AUX_DEV>(rw, oh, 0); SB(); }
      score(d, p); score(e, p + 16);
      p += 32;
    };
    if (HP == 1) kit(true);
    kit(false);
#pragma unroll 1
    for (int it = 2 + HP; it < pairs; ++it) kit(false);
    dots(A, d); dots(B, e);
    dl += PY1 * 256u;
    pace();
    dl += PY2 * 256u;
    const auto rv = rsrc(vh, SK * DH * 2);
    ISSUE2(rv, s0);
    score(d, p); score(e, p + 16);
    TS(10);
  }
  auto quad4 = [&](float a0, float a1, bool up, float (&r)[4]) {
    const float b0 = dpp<XOR1>(a0), b1 = dpp<XOR1>(a1);
    r[0] = up ? b0 : a0; r[1] = up ? b1 : a1; r[2] = up ? a0 : b0; r[3] = up ? a1 : b1;
  };
  m = fmaxf(m, dpp<ROR4>(m)); m = xrow_max(fmaxf(m, dpp<ROR8>(m)));
  __builtin_amdgcn_wave_barrier();
  SB();
  {
    float l = 0.f;
    float* p = sT + w * (ROUNDS * 16) + 4 * lane;
#pragma unroll
    for (int k = 0; k < ROUNDS / 16; ++k) {
      f32x4 e = *(const f32x4*)(p + 256 * k);
#pragma unroll
      for (int t = 0; t < 4; ++t) e[t] = __builtin_amdgcn_exp2f(e[t] - m);
      *(f32x4*)(p + 256 * k) = e;
      l += (e[0] + e[1]) + (e[2] + e[3]);
    }
    l = wave_sum(l);
    if (lane == 0) *(f32x2*)(sM + 2 * w) = f32x2{m, l};
    if (arrive(&sCn3)) {
      const f32x2 r = merge8(*(const f32x2*)(sM + 2 * (lane & 7)));
      if (lane == 0) g_put2(rw, G_M + 2 * b, r, tag);
    }
    SB();
  }
  f32x2 z[4] = {};
  {
    if (w < 7) SPIN1(ha, oh, 2);
    float v0 = __uint_as_float(ha[0]), v1 = __uint_as_float(ha[2]);
    v0 = mrbf(v0 + dpp<XOR2>(v0)); v1 = mrbf(v1 + dpp<XOR2>(v1));
    const float u0 = dpp<HMIR>(v0), u1 = dpp<HMIR>(v1);
    auto glu = [&](float own, float other) { return mrbf(mrbf(1.f / (1.f + __expf(-((j & 4) ? other : own)))) * ((j & 4) ? own : other)); };
    float hv[4];
    quad4(glu(v0, u0), glu(v1, u1), hp, hv);
#pragma unroll
    for (int t = 0; t < 4; ++t) axpy8(z, hv[t], Wd[t]);
#pragma unroll
    for (int t = 0; t < 4; t += 2)
#pragma unroll
      for (int k = 0; k < 4; ++k) {
        const unsigned r0 = Wo[t][k], r1 = Wo[t + 1][k];
        Wo[t][k] = __builtin_amdgcn_perm(r1, r0, 0x05040100); Wo[t + 1][k] = __builtin_amdgcn_perm(r1, r0, 0x07060302);
      }
    asm volatile("" : "+v"(z[0]), "+v"(z[1]), "+v"(z[2]), "+v"(z[3]), "+v"(Wo[0]), "+v"(Wo[1]), "+v"(Wo[2]), "+v"(Wo[3]));
    SB();
  }
  f32x2 acc[4] = {};
  float f, fk[NW];
  {
    unsigned so = s0;
    const auto rv = rsrc(vh, SK * DH * 2);
    const unsigned om = w == 0 ? (G_M + h * 16 + 2 * (lane & 7)) * 8 : OOB;
    u32x4 gm = {};
    auto pv = [&](const float* p, const u32x4 (&VV)[4]) {
      const f32x4 e = *(const f32x4*)p;
#pragma unroll
      for (int t = 0; t < 4; ++t) axpy8(acc, e[t], VV[t]);
      SB();
    };
    auto vit = [&](bool gather) {
      NEXT(so);
      pv(st, A);
      pv(st + 16, B);
      asm volatile("" : "+v"(acc[0]), "+v"(acc[1]), "+v"(acc[2]), "+v"(acc[3]));
      pace();
      ISSUE2(rv, so);
      if (gather) { gm = bl<AUX_DEV>(rw, om, 0); SB(); }
      st += 32;
    };
#pragma unroll 1
    for (int it = 1; it < GE; ++it) vit(false);
    vit(true); vit(false);
#pragma unroll 1
    for (int it = GE + 2; it < pairs; ++it) vit(false);
    if (w == 0) {
      SPIN1(gm, om, 2);
      const f32x2 r = merge8(f32x2{__uint_as_float(gm[0]), __uint_as_float(gm[2])});
      while (__hip_atomic_load(&sCn3, __ATOMIC_RELAXED, __HIP_MEMORY_SCOPE_WORKGROUP) < NW) __builtin_amdgcn_s_sleep(1);
      __builtin_amdgcn_fence(__ATOMIC_ACQUIRE, "workgroup", "local");
      f = __builtin_amdgcn_exp2f(sM[2 * (lane & 7)] - r.x) / r.y;
    } else f = 0.f;
#pragma unroll
    for (int k = 0; k < NW; ++k) { fk[k] = __builtin_bit_cast(float, __builtin_amdgcn_readlane(__builtin_bit_cast(int, f), k)); asm volatile("" : "+s"(fk[k])); }
    SB();
    pv(st, A);
    pv(st + 16, B);
  }
  asm volatile("" :: "v"(pfd));
  TS(6);
  SB();
  *(f32x2*)(sB + w * 128 + 8 * j + 2 * g) = xrow_sum8(acc);
  SB();
  lds_barrier();
  unsigned tB = (unsigned)__builtin_amdgcn_s_memrealtime() * 256u;
  TS(7);
  auto sum8 = [&](const float* p) {
    f32x2 t[NW];
#pragma unroll
    for (int k = 0; k < NW; ++k) t[k] = *(const f32x2*)(p + k * 128 + 2 * lane);
    return ((t[0] + t[1]) + (t[2] + t[3])) + ((t[4] + t[5]) + (t[6] + t[7]));
  };
  if (w == 0) {
    f32x2 t[NW];
#pragma unroll
    for (int k = 0; k < NW; ++k) t[k] = *(const f32x2*)(sB + k * 128 + 2 * lane);
    f32x2 u = t[0] * fk[0], v = t[1] * fk[1];
#pragma unroll
    for (int k = 2; k < NW; k += 2) { u = __builtin_elementwise_fma(t[k], f32x2{fk[k], fk[k]}, u); v = __builtin_elementwise_fma(t[k + 1], f32x2{fk[k + 1], fk[k + 1]}, v); }
    g_put2(rw, G_1 + b * 128 + 2 * lane, u + v, tag);
  }
  SB();
  const unsigned o1 = (G_1 + (hc * NS + (j >> 1)) * 128 + 16 * w + 4 * g + 2 * (j & 1)) * 8;
  until(tB + 36 * 256u);
  until(tS + P8 * 256u);
  u32x4 pa = bl<AUXP>(rw, o1, 0);
  SB();
  SPIN1(pa, o1, 4);
  TS(8);
  {
    const unsigned p0 = cvtpk(sum_p(__uint_as_float(pa[0])), sum_p(__uint_as_float(pa[2])));
    const unsigned p1 = __builtin_amdgcn_mov_dpp(p0, XOR1, 0xf, 0xf, true);
#pragma unroll
    for (int k = 0; k < 4; ++k) { z[k].x = dot2(Wo[0][k], p0, z[k].x); z[k].y = dot2(Wo[1][k], p0, z[k].y); }
#pragma unroll
    for (int k = 0; k < 4; ++k) { z[k].x = dot2(Wo[2][k], p1, z[k].x); z[k].y = dot2(Wo[3][k], p1, z[k].y); }
    *(f32x2*)(sA + w * 128 + 8 * j + 2 * g) = xrow_sum8(z);
  }
  lds_barrier();
  if (w == 0) g_put2(rw, G_2 + (sc * NH + hc) * 128 + 2 * lane, sum8(sA), tag);
  TS(9);
  if (w == 2) {
    const unsigned o2 = (G_2 + (s * NH + (lane >> 1)) * 128 + 4 * h + 2 * (lane & 1)) * 8;
    tB = (unsigned)__builtin_amdgcn_s_memrealtime() * 256u;
    until(tB + 28 * 256u);
    until(tS + P9 * 256u);
    u32x4 fa = bl<AUXP>(rw, o2, 0);
    SPIN1(fa, o2, 1);
    float v0 = __uint_as_float(fa[0]), v1 = __uint_as_float(fa[2]);
    v0 = xrow_sum(sum_p(v0)); v1 = xrow_sum(sum_p(v1));
    if (lane < 2) out[64 * s + 2 * h + lane] = cvtpk(bf_lo(xx) + v0, bf_hi(xx) + v1);
    if (b == 0 && lane == 0) __hip_atomic_store((unsigned*)ws, tag, __ATOMIC_RELAXED, __HIP_MEMORY_SCOPE_AGENT);
  }
  asm volatile(".Lfdb_end:");
  TS(11);
}

extern "C" void kernel_launch(void* const* d_in, const int* in_sizes, int n_in, void* d_out, int out_size, void* d_ws,
                              size_t ws_size, hipStream_t stream) {
  (void)in_sizes; (void)n_in; (void)out_size; (void)ws_size;
  fused_decode_block<<<NWG, NT, 0, stream>>>(d_in[0], d_in[1], d_in[2], d_in[3], d_in[4], d_in[6],
                                              d_in[7], d_in[8], d_out, (char*)d_ws + WSOFF XVALS);
}
```

```cpp
#include <hip/hip_runtime.h>
#include <hip/hip_bf16.h>
#include <stdint.h>

#define MIMIC 1
#define PROF 0

typedef unsigned u32x4 __attribute__((ext_vector_type(4)));
typedef unsigned u32x2 __attribute__((ext_vector_type(2)));
typedef float f32x2 __attribute__((ext_vector_type(2)));
typedef float f32x4 __attribute__((ext_vector_type(4)));
typedef __bf16 bf16x2 __attribute__((ext_vector_type(2)));
typedef unsigned long long u64;
typedef u64 u64x2 __attribute__((ext_vector_type(2)));
typedef u64 u64x8 __attribute__((ext_vector_type(8)));
#define GLOBAL __attribute__((address_space(1)))

constexpr int DM = 1024, NH = 32, SK = 65536, DH = 128, FF = 3584;
constexpr int NS = 8;
constexpr int NW = 8;
constexpr int NT = NW * 64;
constexpr int NWG = NH * NS;
constexpr int FH = FF / NH;
constexpr int ROUNDS = SK / NS / 16 / NW;
constexpr unsigned RBYTES = NW * 4096;
constexpr unsigned OOB = 0x80000000u;
#define AUXK 2
#define AUXV 2
#define AUXP 16
constexpr int AUX_NT = 2, AUX_DEV = 16;
constexpr float LOG2E = 1.4426950408889634f;
constexpr float SCALE = 0.08838834764831845f;
constexpr int SPIN_MAX = 1 << 18;

#define DD 13500
#define TG 150
#define TGW 200
#define TK 590
#define TW0 340
#define TW1 200
#define WSOFF 0
#define TD 80
#define WS 4
#define WE 4
#define TX 80
#define TP1 110
#define TP2 202
#define RM 1
#define TA 8
#define TH 12
#define SG 0
#ifndef CU_
#define CU_ 16
#endif
#define HS 0
#define TY1 0
#define TY2 0
#define PF0 1
#define PF1 7
#define PF2 0
#define LT0 0
#define CT0 1
#define WARM 8
#define ORDER 0
#define HP 1
#define GE 30
#define TPERM 0x50423671
#define APERM 0x7d54f68
#define LPERM 0x30b1
#define XM3 0x1100000
#define XM0 0x98000034
#define XM1 0x1000
#define XM2 0xa0002020
constexpr unsigned SLOT = DD * 256u / 64, TK_ = TK, TG_ = TG, TW0_ = TW0, TW1_ = TW1, TGW_ = TGW, TD_ = TD, P6 = WE, P7 = TX, P8 = TP1, P9 = TP2, P10 = RM, P11 = TA, P12 = TH, P13 = SG, P14 = CU_, P15 = HS, P16 = TPERM, P17 = XM0, P18 = XM1, P19 = XM2, P20 = APERM, P21 = LPERM, P22 = XM3;
#define XARGS
#define XVALS
#define PFA PF0
#define PFB PF1
#define PFC PF2
#define PY1 TY1
#define PY2 TY2
constexpr size_t G_Q = 32;
constexpr size_t G_H = G_Q + NH * 4 * DH;
constexpr size_t G_T = G_H + 4 * FF;
constexpr size_t G_M = G_T + NWG * 2;
constexpr size_t G_1 = G_M + NWG * 2;
constexpr size_t G_2 = G_1 + (size_t)NWG * 128;
constexpr size_t G_END = G_2 + (size_t)NWG * 128;
constexpr size_t WS_PROF = (G_END * 8 + 4095) / 4096 * 4096;

__device__ __forceinline__ float bf_lo(unsigned u) { return __builtin_bit_cast(float, u << 16); }
__device__ __forceinline__ float bf_hi(unsigned u) { return __builtin_bit_cast(float, u & 0xffff0000u); }
__device__ __forceinline__ unsigned cvtpk(float lo, float hi) {
  bf16x2 r = {(__bf16)lo, (__bf16)hi};
  return __builtin_bit_cast(unsigned, r);
}
__device__ __forceinline__ float rbf(float x) { return bf_lo(cvtpk(x, 0.f)); }
__device__ __forceinline__ float mrbf(float x) { return MIMIC ? rbf(x) : x; }
__device__ __forceinline__ float dot2(unsigned a, unsigned b, float c) {
  return __builtin_amdgcn_fdot2_f32_bf16(__builtin_bit_cast(bf16x2, a), __builtin_bit_cast(bf16x2, b), c, false);
}
template <int CTRL> __device__ __forceinline__ float dpp(float x) {
  return __builtin_bit_cast(float, __builtin_amdgcn_mov_dpp(__builtin_bit_cast(int, x), CTRL, 0xf, 0xf, true));
}
constexpr int XOR1 = 0xB1, XOR2 = 0x4E, HMIR = 0x141, ROR8 = 0x128, ROR4 = 0x124, ROR2 = 0x122, ROR1 = 0x121, BCAST = 0x150;
__device__ __forceinline__ float xrow_sum(float x) {
  auto s = __builtin_amdgcn_permlane16_swap(__float_as_uint(x), __float_as_uint(x), false, false);
  x = __uint_as_float(s[0]) + __uint_as_float(s[1]);
  auto t = __builtin_amdgcn_permlane32_swap(__float_as_uint(x), __float_as_uint(x), false, false);
  return __uint_as_float(t[0]) + __uint_as_float(t[1]);
}
__device__ __forceinline__ float xrow_max(float x) {
  auto s = __builtin_amdgcn_permlane16_swap(__float_as_uint(x), __float_as_uint(x), false, false);
  x = fmaxf(__uint_as_float(s[0]), __uint_as_float(s[1]));
  auto t = __builtin_amdgcn_permlane32_swap(__float_as_uint(x), __float_as_uint(x), false, false);
  return fmaxf(__uint_as_float(t[0]), __uint_as_float(t[1]));
}
__device__ __forceinline__ float wave_sum(float x) {
  x += dpp<ROR8>(x); x += dpp<ROR4>(x); x += dpp<ROR2>(x); x += dpp<ROR1>(x);
  return xrow_sum(x);
}
__device__ __forceinline__ void lds_barrier() {
  __builtin_amdgcn_fence(__ATOMIC_RELEASE, "workgroup", "local");
  __builtin_amdgcn_s_barrier();
  __builtin_amdgcn_fence(__ATOMIC_ACQUIRE, "workgroup", "local");
}
__device__ __forceinline__ void g_put(u64* p, float v, unsigned tag) {
  __hip_atomic_store(p, ((u64)tag << 32) | __float_as_uint(v), __ATOMIC_RELAXED, __HIP_MEMORY_SCOPE_AGENT);
}
__device__ __forceinline__ void g_put2(__amdgpu_buffer_rsrc_t r, unsigned granule, unsigned a, unsigned b, unsigned tag) {
  __builtin_amdgcn_raw_buffer_store_b128(u32x4{a, tag, b, tag}, r, granule * 8, 0, AUX_DEV);
}
__device__ __forceinline__ void g_put2(__amdgpu_buffer_rsrc_t r, unsigned granule, f32x2 v, unsigned tag) {
  g_put2(r, granule, __float_as_uint(v.x), __float_as_uint(v.y), tag);
}
__device__ __forceinline__ bool g_ok(u32x4 g, unsigned tag) { return g[1] == tag && g[3] == tag; }
__device__ __forceinline__ __amdgpu_buffer_rsrc_t rsrc(const void* p, unsigned bytes) {
  return __builtin_amdgcn_make_buffer_rsrc((void*)p, 0, (int)bytes, 0x00020000);
}
template <int AUX> __device__ __forceinline__ u32x4 bl(__amdgpu_buffer_rsrc_t r, unsigned voff, unsigned soff) {
  return __builtin_amdgcn_raw_buffer_load_b128(r, voff, soff, AUX);
}
__device__ __forceinline__ void axpy8(f32x2 (&acc)[4], float a, u32x4 w) {
  const f32x2 aa = {a, a};
#pragma unroll
  for (int k = 0; k < 4; ++k) { const f32x2 v = {bf_lo(w[k]), bf_hi(w[k])}; acc[k] = __builtin_elementwise_fma(v, aa, acc[k]); }
}
__device__ __forceinline__ f32x2 xrow_sum8(const f32x2 (&a)[4]) {
  auto h32 = [](float p, float q) {
    auto s = __builtin_amdgcn_permlane32_swap(__float_as_uint(p), __float_as_uint(q), false, false);
    return __uint_as_float(s[0]) + __uint_as_float(s[1]); };
  auto h16 = [](float p, float q) {
    auto s = __builtin_amdgcn_permlane16_swap(__float_as_uint(p), __float_as_uint(q), false, false);
    return __uint_as_float(s[0]) + __uint_as_float(s[1]); };
  return f32x2{h16(h32(a[0].x, a[2].x), h32(a[1].x, a[3].x)), h16(h32(a[0].y, a[2].y), h32(a[1].y, a[3].y))};
}
__device__ __forceinline__ float sum_p(float x) { x += dpp<ROR2>(x); x += dpp<ROR4>(x); return x + dpp<ROR8>(x); }
__device__ __forceinline__ f32x2 merge8(f32x2 v) {
  float M = fmaxf(v.x, dpp<XOR1>(v.x)); M = fmaxf(M, dpp<XOR2>(M)); M = fmaxf(M, dpp<HMIR>(M));
  float L = v.y * __builtin_amdgcn_exp2f(v.x - M);
  L += dpp<XOR1>(L); L += dpp<XOR2>(L); L += dpp<HMIR>(L);
  return f32x2{M, L};
}
#define SB() __builtin_amdgcn_sched_barrier(0)
#define VMWAIT(n) do { __builtin_amdgcn_s_waitcnt(0xF70 | (n)); SB(); } while (0)
#define SPIN2(a, oa, c, oc, slp) do { if (!__all(g_ok(a, tag) && g_ok(c, tag))) { _Pragma("unroll 1") for (int it = 0; it < SPIN_MAX; ++it) { \
    asm volatile("" ::: "memory"); a = bl<AUX_DEV>(rw, oa, 0); c = bl<AUX_DEV>(rw, oc, 0); \
    if (__all(g_ok(a, tag) && g_ok(c, tag))) break; __builtin_amdgcn_s_sleep(slp); } } } while (0)
#define SPIN1(a, oa, slp) do { if (!__all(g_ok(a, tag))) { _Pragma("unroll 1") for (int it = 0; it < SPIN_MAX; ++it) { \
    asm volatile("" ::: "memory"); a = bl<AUXP>(rw, oa, 0); \
    if (__all(g_ok(a, tag))) break; __builtin_amdgcn_s_sleep(slp); } } } while (0)
#define TS(i) do {} while (0)

__global__ void __launch_bounds__(NT, 2)
fused_decode_block(const void*, const void*, const void*, const void*, const void*, const void*, const void*, const void*, void*, void* XARGS) {
  __shared__ __attribute__((aligned(16))) float sA[NW * 128];
  __shared__ __attribute__((aligned(16))) float sB[NW * 128];
  __shared__ int sCnt, sCn2, sCn3, sQf;
  __shared__ __attribute__((aligned(16))) unsigned sQ[68];
  __shared__ __attribute__((aligned(16))) float sX[DM];
  __shared__ __attribute__((aligned(16))) float sM[2 * NW];
  __shared__ __attribute__((aligned(16))) float sT[NW * ROUNDS * 16];
  const int tid = threadIdx.x, lane = tid & 63, g = lane >> 4, j = lane & 15;
  const int w = __builtin_amdgcn_readfirstlane(tid >> 6);
  const int b = blockIdx.x, s = b & 7, h = b >> 3;
  const int hc = P10 ? 4 * s + (h & 3) : h, sc = P10 ? h >> 2 : s;
  unsigned pfd;
  u64x8 ka; u64x2 kb;
  {
    u64 pc;
    asm volatile("s_load_dwordx16 %2, %4, 0x0\n\ts_load_dwordx4 %3, %4, 0x40\n\ts_getpc_b64 %1\n.Lpf%=:\n\tv_mul_u32_u24 %0, %6, %5\n\t"
                 "v_min_u32 %0, .Lfdb_end-.Lpf%=, %0\n\tglobal_load_dword %0, %0, %1\n\ts_waitcnt lgkmcnt(0)"
                 : "=&v"(pfd), "=&s"(pc), "=&s"(ka), "=&s"(kb) : "s"(__builtin_amdgcn_kernarg_segment_ptr()), "v"(tid), "s"(PFA && !(h & PFB) ? 64 : 0));
  }
  SB();
  const unsigned short* x = (const unsigned short*)(const GLOBAL unsigned short*)ka[0];
  const void *kc = (const void*)(const GLOBAL char*)ka[1], *vc = (const void*)(const GLOBAL char*)ka[2], *wq = (const void*)(const GLOBAL char*)ka[3],
             *wo = (const void*)(const GLOBAL char*)ka[4], *wg = (const void*)(const GLOBAL char*)ka[5], *wu = (const void*)(const GLOBAL char*)ka[6],
             *wd = (const void*)(const GLOBAL char*)ka[7];
  unsigned* out = (unsigned*)(GLOBAL unsigned*)kb[0];
  u64* ws = (u64*)(GLOBAL u64*)kb[1];
  TS(0);
  unsigned t0 = (unsigned)__builtin_amdgcn_s_memrealtime() * 256u;
  const unsigned idx = w * 32 + (h & 3) * 8 + s;
  auto until = [](unsigned t) {
    while ((int)((unsigned)__builtin_amdgcn_s_memrealtime() * 256u - t) < 0) __builtin_amdgcn_s_sleep(1);
    SB();
  };
  const u32x4 xa = *(const u32x4*)(x + 16 * lane), xb = *(const u32x4*)(x + 16 * lane + 8);
  const unsigned xo = *(const unsigned*)(x + 2 * tid);
  unsigned xx = *(const unsigned*)(x + 128 * s + 4 * h + 2 * (lane & 1));
  const auto rw = rsrc(ws, G_END * 8);
  const unsigned epoch = __builtin_amdgcn_raw_buffer_load_b32(rw, 0, 0, 0);
  u32x4 W[4];
  const int rowq = 256 * (s >> 1) + 32 * w + (lane >> 3);
  {
    const auto rq = rsrc(wq, DM * NH * DH * 2);
#pragma unroll
    for (int t = 0; t < 4; ++t) W[t] = bl<AUX_NT>(rq, rowq * (NH * DH * 2) + (2 * h + (s & 1)) * 128 + (lane & 7) * 16, t * 8 * NH * DH * 2);
  }
  SB();
  const char* kh = (const char*)kc + (size_t)h * SK * DH * 2;
  const char* vh = (const char*)vc + (size_t)h * SK * DH * 2;
  const auto rk = rsrc(kh, SK * DH * 2);
  unsigned warm = 0;
  if (w == WARM - 1 && lane < (int)P14) {
    const char* a = P14 > 8 ? (lane & 8 ? kh : vh) : lane < 2 ? vh : kh;
    size_t o = P14 > 8 ? (size_t)(lane & 7) * (ROUNDS * RBYTES) + s * 4096 : (size_t)(s + (lane & 1)) * (ROUNDS * RBYTES) - (lane & 1) * 64;
    warm = __builtin_nontemporal_load((const unsigned*)(a + o));
  }
  SB();
  u32x4 A[4], B[4];
  auto at = [](unsigned p, int i) { return 256u << ((p >> 4 * i) & 15); };
  const unsigned kvo = j * 16 + (g & 1) * at(P21, 0) + (g >> 1) * at(P21, 1);
  const unsigned tb0 = at(P21, 2), tb1 = at(P21, 3), abit = at(P20, 3);
#define ISSUE2(rr, so) do { _Pragma("unroll") for (int t = 0; t < 4; ++t) A[t] = bl<AUXK>(rr, kvo, (so) ^ (t & 1) * tb0 ^ (t >> 1) * tb1); SB(); \
    _Pragma("unroll") for (int t = 0; t < 4; ++t) B[t] = bl<AUXK>(rr, kvo, (so) ^ abit ^ (t & 1) * tb0 ^ (t >> 1) * tb1); SB(); } while (0)
#define NEXT(so) (cnt = ((cnt | fixm) + 1) & ~fixm, so = s0 ^ cnt)
  unsigned s0 = 0, fixm = 255 | at(P21, 0) | at(P21, 1) | tb0 | tb1;
#pragma unroll
  for (int i = 0; i < 7; ++i) { fixm |= at(P20, i); s0 |= (((w | s << 4) >> i) & 1) * at(P20, i); }
#pragma unroll
  for (int i = 0; i < 4; ++i) s0 ^= (__builtin_popcount(b & (P22 >> 8 * i) & 255) & 1) << (8 + i) | (__builtin_popcount(b & (P17 >> 8 * i) & 255) & 1) << (12 + i)
                                  | (__builtin_popcount(b & (P18 >> 8 * i) & 255) & 1) << (16 + i) | (__builtin_popcount(b & (P19 >> 8 * i) & 255) & 1) << (20 + i);
  s0 &= ~(at(P21, 0) | at(P21, 1));
  constexpr int pairs = ROUNDS / 2;
  unsigned pos = w * 256;
#pragma unroll
  for (int i = 0; i < 8; ++i) pos |= ((b >> ((P16 >> 4 * i) & 15)) & 1) << i;
  unsigned dl, slot = SLOT;
  auto pace = [&]() {
    until(dl);
    dl += slot;
  };
  if (tid == 0) { sCnt = 0; sCn2 = 0; sCn3 = 0; sQf = 0; sQ[65] = t0; }
  {
    float v[16];
#pragma unroll
    for (int k = 0; k < 4; ++k) { v[2 * k] = bf_lo(xa[k]); v[2 * k + 1] = bf_hi(xa[k]); v[8 + 2 * k] = bf_lo(xb[k]); v[9 + 2 * k] = bf_hi(xb[k]); }
    float sum = 0.f;
#pragma unroll
    for (int k = 0; k < 16; ++k) sum += v[k];
    const float mean = wave_sum(sum) * (1.f / DM);
    float sq = 0.f;
#pragma unroll
    for (int k = 0; k < 16; ++k) { const float d = v[k] - mean; sq = fmaf(d, d, sq); }
    const float var = wave_sum(sq) * (1.f / DM);
    const float mb = mrbf(mean), den = mrbf(__builtin_sqrtf(mrbf(mrbf(var) + 1e-5f)));
    *(f32x2*)(sX + 2 * tid) = f32x2{mrbf(mrbf(bf_lo(xo) - mb) / den), mrbf(mrbf(bf_hi(xo) - mb) / den)};
  }
  const unsigned tag = __builtin_amdgcn_readfirstlane(epoch) + 1u;
  asm volatile("" : "+v"(xx));
  SB();
  lds_barrier();
  SB();
  if (CT0) t0 = __builtin_amdgcn_readfirstlane(sQ[65]);
  dl = t0 + TK_ * 256u + (SLOT * pos >> 11);
  TS(1);
  auto arrive = [&](int* cnt) {
    __builtin_amdgcn_fence(__ATOMIC_RELEASE, "workgroup", "local");
    int old = 0;
    if (lane == 0) old = __hip_atomic_fetch_add(cnt, 1, __ATOMIC_RELAXED, __HIP_MEMORY_SCOPE_WORKGROUP);
    const bool fin = __builtin_amdgcn_readfirstlane(old) == NW - 1;
    __builtin_amdgcn_fence(__ATOMIC_ACQUIRE, "workgroup", "local");
    return fin;
  };
  auto deliver = [&](const f32x2 (&a)[4], float* dst, int* cnt) {
    f32x2 r = xrow_sum8(a);
    r.x += dpp<ROR8>(r.x); r.y += dpp<ROR8>(r.y);
    if (j < 8) *(f32x2*)(dst + w * 64 + 8 * j + 2 * g) = r;
    return arrive(cnt);
  };
  auto qpart = [&]() {
    f32x2 a[4] = {};
    asm volatile("" : "+v"(W[0]), "+v"(W[1]), "+v"(W[2]), "+v"(W[3]));
#pragma unroll
    for (int t = 0; t < 4; ++t) axpy8(a, sX[rowq + 8 * t], W[t]);
    if (deliver(a, sA, &sCnt) && lane < 33) {
      f32x2 t = {};
#pragma unroll
      for (int k = 0; k < NW; ++k) t += *(const f32x2*)(sA + k * 64 + 2 * (lane & 31));
      if (lane == 32) t.x = __uint_as_float(t0);
      g_put2(rw, lane < 32 ? G_Q + b * 64 + 2 * lane : G_T + 2 * b, t, tag);
    }
    SB();
  };
  const int part = h >> 3, rank = h & 7;
  const unsigned col = 7 * s + rank;
  const int rowg = 512 * (part & 1) + 64 * w + (lane >> 3);
  u32x4 X[8];
  unsigned xvo = rowg * (FF * 2) + col * 128 + (lane & 7) * 16;
  asm volatile("" : "+v"(xvo));
  auto xissue = [&]() {
    const auto rx = rsrc(part < 2 ? wg : wu, rank < 7 ? DM * FF * 2 : 0);
    until(t0 + TG_ * 256u + TGW_ * idx);
#pragma unroll
    for (int t = 0; t < 8; ++t) X[t] = bl<AUX_NT>(rx, xvo, t * 8 * FF * 2);
    SB();
  };
  if (w < (int)P6) { xissue(); qpart(); TS(2); } else { qpart(); TS(2); xissue(); }
  {
    f32x2 a[4] = {};
#pragma unroll
    for (int t = 0; t < 8; ++t) axpy8(a, sX[rowg + 8 * t], X[t]);
    if (deliver(a, sA + 512, &sCn2) && lane < 32 && rank < 7) {
      f32x2 t = {};
#pragma unroll
      for (int k = 0; k < NW; ++k) t += *(const f32x2*)(sA + 512 + k * 64 + 2 * lane);
      g_put2(rw, G_H + part * FF + col * 64 + 2 * lane, t, tag);
    }
    SB();
  }
  asm volatile("" :: "v"(warm));
  TS(3);
  u32x4 Wd[4], Wo[4];
  {
    const unsigned od = w < 7 ? (hc * FH + 16 * w + 4 * g) * (DM * 2) + sc * 256 + j * 16 : OOB;
    const unsigned oo = (hc * DH + 16 * w + 4 * g) * (DM * 2) + sc * 256 + j * 16, op = (j & 1) * 2 * (DM * 2);
    const auto rd = rsrc(wd, FF * DM * 2), ro = rsrc(wo, NH * DH * DM * 2);
    until(t0 + TW0_ * 256u + TW1_ * idx);
#pragma unroll
    for (int t = 0; t < 4; ++t) Wd[t] = bl<AUX_NT>(rd, od, t * DM * 2);
#pragma unroll
    for (int t = 0; t < 4; ++t) Wo[t] = bl<AUX_NT>(ro, (oo + (t & 2) * (DM * 2)) ^ op, (t & 1) * DM * 2);
    SB();
  }
  const unsigned oq = w == 0 ? (G_Q + h * 512 + 2 * lane) * 8 : OOB;
  until(dl - TD_ * 256u);
  u32x4 qa = bl<AUX_DEV>(rw, oq, 0), qb = bl<AUX_DEV>(rw, oq, 1024), qc = bl<AUX_DEV>(rw, oq, 2048), qd = bl<AUX_DEV>(rw, oq, 3072);
  const unsigned ot = w == 0 ? (G_T + (P11 & 8 ? 0 : h * 16) + 2 * (lane & 7)) * 8 : OOB;
  u32x4 qt = bl<AUX_DEV>(rw, ot, 0);
  SB();
  pace(); dl += P7 * 256u;
  ISSUE2(rk, s0);
  const int hp = (j ^ (j >> 2)) & 1;
  const unsigned oh = w < 7 ? (G_H + ((j >> 1) & 3) * FF + hc * FH + 16 * w + 4 * g + 2 * hp) * 8 : OOB;
  u32x4 ha = {};
  if (HP == 0) { ha = bl<AUX_DEV>(rw, oh, 0); SB(); }
  TS(4);
  if (w == 0) {
    SPIN2(qa, oq, qb, oq + 1024, 2);
    SPIN2(qc, oq + 2048, qd, oq + 3072, 2);
    SPIN1(qt, ot, 2);
    int dt = qt[0] - t0;
    int mn = min(max(dt, -400 * 256), 400 * 256), mx = mn;
    mn = min(mn, __builtin_amdgcn_mov_dpp(mn, XOR1, 0xf, 0xf, true)); mn = min(mn, __builtin_amdgcn_mov_dpp(mn, XOR2, 0xf, 0xf, true));
    mn = min(mn, __builtin_amdgcn_mov_dpp(mn, HMIR, 0xf, 0xf, true));
    mx = max(mx, __builtin_amdgcn_mov_dpp(mx, XOR1, 0xf, 0xf, true)); mx = max(mx, __builtin_amdgcn_mov_dpp(mx, XOR2, 0xf, 0xf, true));
    mx = max(mx, __builtin_amdgcn_mov_dpp(mx, HMIR, 0xf, 0xf, true));
    if (lane == 0) sQ[64] = mn + ((mx - mn) * P12 >> 4) ;
    sQ[lane] = cvtpk((__uint_as_float(qa[0]) + __uint_as_float(qb[0])) + (__uint_as_float(qc[0]) + __uint_as_float(qd[0])),
                     (__uint_as_float(qa[2]) + __uint_as_float(qb[2])) + (__uint_as_float(qc[2]) + __uint_as_float(qd[2])));
    __builtin_amdgcn_fence(__ATOMIC_RELEASE, "workgroup", "local");
    if (lane == 0) __hip_atomic_store(&sQf, 1, __ATOMIC_RELAXED, __HIP_MEMORY_SCOPE_WORKGROUP);
  }
  while (__hip_atomic_load(&sQf, __ATOMIC_RELAXED, __HIP_MEMORY_SCOPE_WORKGROUP) == 0) __builtin_amdgcn_s_sleep(1);
  __builtin_amdgcn_fence(__ATOMIC_ACQUIRE, "workgroup", "local");
  u32x4 q = *(const u32x4*)(sQ + 4 * j);
  const unsigned shift = __builtin_amdgcn_readfirstlane(sQ[64]);
  dl += shift;
  const unsigned tS = t0 + shift + (TK_ + P7 + PY1 + PY2) * 256u + 64 * SLOT;
  asm volatile("" : "+v"(q));
  TS(5);
  SB();
  float m = -1e30f;
  float* st = sT + w * (ROUNDS * 16) + 4 * g;
  auto dots = [&](const u32x4 (&KK)[4], float (&d)[4]) {
#pragma unroll
    for (int t = 0; t < 4; ++t) d[t] = dot2(KK[t][3], q[3], dot2(KK[t][2], q[2], dot2(KK[t][1], q[1], dot2(KK[t][0], q[0], 0.f))));
    asm volatile("" : "+v"(d[0]), "+v"(d[1]), "+v"(d[2]), "+v"(d[3]));
    SB();
  };
  auto score = [&](const float (&d)[4], float* p) {
    const float y0 = d[0] + dpp<ROR8>(d[0]), y1 = d[1] + dpp<ROR8>(d[1]), y2 = d[2] + dpp<ROR8>(d[2]), y3 = d[3] + dpp<ROR8>(d[3]);
    const float ya = (j & 8) ? y2 : y0, yb = (j & 8) ? y3 : y1;
    const float za = ya + dpp<HMIR>(ya), zb = yb + dpp<HMIR>(yb);
    float sc = (j & 4) ? zb : za;
    sc += dpp<XOR1>(sc); sc += dpp<XOR2>(sc);
    const float t2 = MIMIC ? rbf(rbf(sc) * SCALE) * LOG2E : sc * (SCALE * LOG2E);
    m = fmaxf(m, t2);
    if ((j & 3) == 0) p[j >> 2] = t2;
    SB();
  };
  {
    unsigned so = s0, cnt = 0;
    float* p = st;
    float d[4], e[4];
    auto kit = [&](bool gather) {
      NEXT(so);
      dots(A, d); dots(B, e);
      pace();
      ISSUE2(rk, so);
      if (gather) { ha = bl<AUX_DEV>(rw, oh, 0); SB(); }
      score(d, p); score(e, p + 16);
      p += 32;
    };
    if (HP == 1) kit(true);
    kit(false);
#pragma unroll 1
    for (int it = 2 + HP; it < pairs; ++it) kit(false);
    dots(A, d); dots(B, e);
    dl += PY1 * 256u;
    pace();
    dl += PY2 * 256u;
    const auto rv = rsrc(vh, SK * DH * 2);
    ISSUE2(rv, s0);
    score(d, p); score(e, p + 16);
    TS(10);
  }
  auto quad4 = [&](float a0, float a1, bool up, float (&r)[4]) {
    const float b0 = dpp<XOR1>(a0), b1 = dpp<XOR1>(a1);
    r[0] = up ? b0 : a0; r[1] = up ? b1 : a1; r[2] = up ? a0 : b0; r[3] = up ? a1 : b1;
  };
  m = fmaxf(m, dpp<ROR4>(m)); m = xrow_max(fmaxf(m, dpp<ROR8>(m)));
  __builtin_amdgcn_wave_barrier();
  SB();
  {
    float l = 0.f;
    float* p = sT + w * (ROUNDS * 16) + 4 * lane;
#pragma unroll
    for (int k = 0; k < ROUNDS / 16; ++k) {
      f32x4 e = *(const f32x4*)(p + 256 * k);
#pragma unroll
      for (int t = 0; t < 4; ++t) e[t] = __builtin_amdgcn_exp2f(e[t] - m);
      *(f32x4*)(p + 256 * k) = e;
      l += (e[0] + e[1]) + (e[2] + e[3]);
    }
    l = wave_sum(l);
    if (lane == 0) *(f32x2*)(sM + 2 * w) = f32x2{m, l};
    if (arrive(&sCn3)) {
      const f32x2 r = merge8(*(const f32x2*)(sM + 2 * (lane & 7)));
      if (lane == 0) g_put2(rw, G_M + 2 * b, r, tag);
    }
    SB();
  }
  f32x2 z[4] = {};
  {
    if (w < 7) SPIN1(ha, oh, 2);
    float v0 = __uint_as_float(ha[0]), v1 = __uint_as_float(ha[2]);
    v0 = mrbf(v0 + dpp<XOR2>(v0)); v1 = mrbf(v1 + dpp<XOR2>(v1));
    const float u0 = dpp<HMIR>(v0), u1 = dpp<HMIR>(v1);
    auto glu = [&](float own, float other) { return mrbf(mrbf(1.f / (1.f + __expf(-((j & 4) ? other : own)))) * ((j & 4) ? own : other)); };
    float hv[4];
    quad4(glu(v0, u0), glu(v1, u1), hp, hv);
#pragma unroll
    for (int t = 0; t < 4; ++t) axpy8(z, hv[t], Wd[t]);
#pragma unroll
    for (int t = 0; t < 4; t += 2)
#pragma unroll
      for (int k = 0; k < 4; ++k) {
        const unsigned r0 = Wo[t][k], r1 = Wo[t + 1][k];
        Wo[t][k] = __builtin_amdgcn_perm(r1, r0, 0x05040100); Wo[t + 1][k] = __builtin_amdgcn_perm(r1, r0, 0x07060302);
      }
    asm volatile("" : "+v"(z[0]), "+v"(z[1]), "+v"(z[2]), "+v"(z[3]), "+v"(Wo[0]), "+v"(Wo[1]), "+v"(Wo[2]), "+v"(Wo[3]));
    SB();
  }
  f32x2 acc[4] = {};
  float f, fk[NW];
  {
    unsigned so = s0, cnt = 0;
    const auto rv = rsrc(vh, SK * DH * 2);
    const unsigned om = w == 0 ? (G_M + h * 16 + 2 * (lane & 7)) * 8 : OOB;
    u32x4 gm = {};
    auto pv = [&](const float* p, const u32x4 (&VV)[4]) {
      const f32x4 e = *(const f32x4*)p;
#pragma unroll
      for (int t = 0; t < 4; ++t) axpy8(acc, e[t], VV[t]);
      SB();
    };
    auto vit = [&](bool gather) {
      NEXT(so);
      pv(st, A);
      pv(st + 16, B);
      asm volatile("" : "+v"(acc[0]), "+v"(acc[1]), "+v"(acc[2]), "+v"(acc[3]));
      pace();
      ISSUE2(rv, so);
      if (gather) { gm = bl<AUX_DEV>(rw, om, 0); SB(); }
      st += 32;
    };
#pragma unroll 1
    for (int it = 1; it < GE; ++it) vit(false);
    vit(true); vit(false);
#pragma unroll 1
    for (int it = GE + 2; it < pairs; ++it) vit(false);
    if (w == 0) {
      SPIN1(gm, om, 2);
      const f32x2 r = merge8(f32x2{__uint_as_float(gm[0]), __uint_as_float(gm[2])});
      while (__hip_atomic_load(&sCn3, __ATOMIC_RELAXED, __HIP_MEMORY_SCOPE_WORKGROUP) < NW) __builtin_amdgcn_s_sleep(1);
      __builtin_amdgcn_fence(__ATOMIC_ACQUIRE, "workgroup", "local");
      f = __builtin_amdgcn_exp2f(sM[2 * (lane & 7)] - r.x) / r.y;
    } else f = 0.f;
#pragma unroll
    for (int k = 0; k < NW; ++k) { fk[k] = __builtin_bit_cast(float, __builtin_amdgcn_readlane(__builtin_bit_cast(int, f), k)); asm volatile("" : "+s"(fk[k])); }
    SB();
    pv(st, A);
    pv(st + 16, B);
  }
  asm volatile("" :: "v"(pfd));
  TS(6);
  SB();
  *(f32x2*)(sB + w * 128 + 8 * j + 2 * g) = xrow_sum8(acc);
  SB();
  lds_barrier();
  unsigned tB = (unsigned)__builtin_amdgcn_s_memrealtime() * 256u;
  TS(7);
  auto sum8 = [&](const float* p) {
    f32x2 t[NW];
#pragma unroll
    for (int k = 0; k < NW; ++k) t[k] = *(const f32x2*)(p + k * 128 + 2 * lane);
    return ((t[0] + t[1]) + (t[2] + t[3])) + ((t[4] + t[5]) + (t[6] + t[7]));
  };
  if (w == 0) {
    f32x2 t[NW];
#pragma unroll
    for (int k = 0; k < NW; ++k) t[k] = *(const f32x2*)(sB + k * 128 + 2 * lane);
    f32x2 u = t[0] * fk[0], v = t[1] * fk[1];
#pragma unroll
    for (int k = 2; k < NW; k += 2) { u = __builtin_elementwise_fma(t[k], f32x2{fk[k], fk[k]}, u); v = __builtin_elementwise_fma(t[k + 1], f32x2{fk[k + 1], fk[k + 1]}, v); }
    g_put2(rw, G_1 + b * 128 + 2 * lane, u + v, tag);
  }
  SB();
  const unsigned o1 = (G_1 + (hc * NS + (j >> 1)) * 128 + 16 * w + 4 * g + 2 * (j & 1)) * 8;
  until(tB + 36 * 256u);
  until(tS + P8 * 256u);
  u32x4 pa = bl<AUXP>(rw, o1, 0);
  SB();
  SPIN1(pa, o1, 4);
  TS(8);
  {
    const unsigned p0 = cvtpk(sum_p(__uint_as_float(pa[0])), sum_p(__uint_as_float(pa[2])));
    const unsigned p1 = __builtin_amdgcn_mov_dpp(p0, XOR1, 0xf, 0xf, true);
#pragma unroll
    for (int k = 0; k < 4; ++k) { z[k].x = dot2(Wo[0][k], p0, z[k].x); z[k].y = dot2(Wo[1][k], p0, z[k].y); }
#pragma unroll
    for (int k = 0; k < 4; ++k) { z[k].x = dot2(Wo[2][k], p1, z[k].x); z[k].y = dot2(Wo[3][k], p1, z[k].y); }
    *(f32x2*)(sA + w * 128 + 8 * j + 2 * g) = xrow_sum8(z);
  }
  lds_barrier();
  if (w == 0) g_put2(rw, G_2 + (sc * NH + hc) * 128 + 2 * lane, sum8(sA), tag);
  TS(9);
  if (w == 2) {
    const unsigned o2 = (G_2 + (s * NH + (lane >> 1)) * 128 + 4 * h + 2 * (lane & 1)) * 8;
    tB = (unsigned)__builtin_amdgcn_s_memrealtime() * 256u;
    until(tB + 28 * 256u);
    until(tS + P9 * 256u);
    u32x4 fa = bl<AUXP>(rw, o2, 0);
    SPIN1(fa, o2, 1);
    float v0 = __uint_as_float(fa[0]), v1 = __uint_as_float(fa[2]);
    v0 = xrow_sum(sum_p(v0)); v1 = xrow_sum(sum_p(v1));
    if (lane < 2) out[64 * s + 2 * h + lane] = cvtpk(bf_lo(xx) + v0, bf_hi(xx) + v1);
    if (b == 0 && lane == 0) __hip_atomic_store((unsigned*)ws, tag, __ATOMIC_RELAXED, __HIP_MEMORY_SCOPE_AGENT);
  }
  asm volatile(".Lfdb_end:");
  TS(11);
}

extern "C" void kernel_launch(void* const* d_in, const int* in_sizes, int n_in, void* d_out, int out_size, void* d_ws,
                              size_t ws_size, hipStream_t stream) {
  (void)in_sizes; (void)n_in; (void)out_size; (void)ws_size;
  fused_decode_block<<<NWG, NT, 0, stream>>>(d_in[0], d_in[1], d_in[2], d_in[3], d_in[4], d_in[6],
                                              d_in[7], d_in[8], d_out, (char*)d_ws + WSOFF XVALS);
}
```
